```python
import torch

def _softmax(x, axis=-1):
    return torch.softmax(x, dim=axis)
def _gelu(x):
    return torch.nn.functional.gelu(x, approximate="tanh")
def _silu(x):
    return torch.nn.functional.silu(x)
def _sigmoid(x):
    return torch.sigmoid(x)
def _layernorm(x, axis=-1, eps=1e-5):
    m = x.mean(dim=axis, keepdim=True)
    v = x.var(dim=axis, keepdim=True, unbiased=False)
    return (x - m) / torch.sqrt(v + eps)
def _scatter_add(tgt, idx, src, axis=0):
    return tgt.clone().index_add_(axis, idx, src)
def _segment_sum(src, seg, n):
    out = torch.zeros((n,) + src.shape[1:], dtype=src.dtype)
    return out.index_add_(0, seg, src)
def _pad(x, pw):
    flat = tuple(v for pair in reversed(pw) for v in pair)
    return torch.nn.functional.pad(x, flat)

kernel_name = 'parallel_sum__softmax_full_mha__glu_gated_geglu__prompt'

def setup_inputs():

    _w = (lambda a: (lambda t: t.to(torch.bfloat16) if t.is_floating_point() else t)(torch.as_tensor(a)))
    def _A_setup():
        B, S, D = 1, 2048, 512
        return {
            "x": _w((lambda *s: torch.randn(*s, dtype=torch.bfloat16) * 0.05)(B, S, D)),
            "wqkv": _w((lambda *s: torch.randn(*s, dtype=torch.bfloat16) * 0.05)(D, 3 * D)),
            "wo": _w((lambda *s: torch.randn(*s, dtype=torch.bfloat16) * 0.05)(D, D)),
        }
    _A_in = _A_setup()

    def _B_setup():
        B, S, Dm, Df = 1, 2048, 512, 1792
        return {
            "x": _w((lambda *s: torch.randn(*s, dtype=torch.bfloat16) * 0.05)(B, S, Dm)),
            "w_gate": _w((lambda *s: torch.randn(*s, dtype=torch.bfloat16) * 0.4)(Dm, Df) * (Dm ** -0.5)),
            "w_up": _w((lambda *s: torch.randn(*s, dtype=torch.bfloat16) * 0.4)(Dm, Df) * (Dm ** -0.5)),
            "w_down": _w((lambda *s: torch.randn(*s, dtype=torch.bfloat16) * 0.4)(Df, Dm) * (Df ** -0.5)),
        }
    _B_in = _B_setup()

    return {
        "A_x": _A_in["x"],
        "A_wqkv": _A_in["wqkv"],
        "A_wo": _A_in["wo"],
        "B_x": _B_in["x"],
        "B_w_gate": _B_in["w_gate"],
        "B_w_up": _B_in["w_up"],
        "B_w_down": _B_in["w_down"],
    }

def reference(A_x, A_wqkv, A_wo, B_x, B_w_gate, B_w_up, B_w_down):
    def _A_ref(x, wqkv, wo):
        B, S, D = x.shape
        H = 32; Dh = D // H
        qkv = (lambda x, *d: x.permute(*d))((x @ wqkv).reshape(B, S, 3, H, Dh), 2, 0, 3, 1, 4)
        q, k, v = qkv[0], qkv[1], qkv[2]
        scale = Dh ** -0.5
        scores = torch.einsum("bhqd,bhkd->bhqk", q, k) * scale
        p = _softmax(scores, axis=-1)
        o = torch.einsum("bhqk,bhkd->bhqd", p, v)
        return (lambda x, *d: x.permute(*d))(o, 0, 2, 1, 3).reshape(B, S, D) @ wo

    def _B_ref(x, w_gate, w_up, w_down):
        g = torch.einsum("bsd,df->bsf", x, w_gate)
        u = torch.einsum("bsd,df->bsf", x, w_up)
        h = _gelu(g) * u
        return torch.einsum("bsf,fd->bsd", h, w_down)

    x = A_x
    xn = _layernorm(x, axis=-1)
    return x + _A_ref(xn, A_wqkv, A_wo) + _B_ref(xn, B_w_gate, B_w_up, B_w_down)
```

```cpp
#include <hip/hip_runtime.h>
#include <hip/hip_bf16.h>
#include <stdint.h>

typedef __bf16 bf16x8 __attribute__((ext_vector_type(8)));
typedef __bf16 bf16x16 __attribute__((ext_vector_type(16)));
typedef float f32x16 __attribute__((ext_vector_type(16)));
typedef float f32x8 __attribute__((ext_vector_type(8)));
typedef float f32x4 __attribute__((ext_vector_type(4)));
typedef float f32x2 __attribute__((ext_vector_type(2)));
typedef unsigned u32x4 __attribute__((ext_vector_type(4)));
typedef unsigned u32x2 __attribute__((ext_vector_type(2)));
typedef short s16x4 __attribute__((ext_vector_type(4)));
typedef __attribute__((address_space(3))) char lds_char;

constexpr int SEQ = 2048, DM = 512, NH = 32, FF = 1792;
constexpr size_t WS_XN = 0;
constexpr size_t WS_Q = WS_XN + (2u << 20);
constexpr size_t WS_KV = WS_Q + (2u << 20);
constexpr size_t WS_KMAX = WS_KV + (4u << 20) + 65536;
constexpr size_t WS_H = WS_KMAX + 65536;
constexpr size_t WS_O = WS_H + (8u << 20);

#define SBAR() __builtin_amdgcn_sched_barrier(0)
#define MFMA32(a, b, c) __builtin_amdgcn_mfma_f32_32x32x16_bf16(a, b, c, 0, 0, 0)
#define WAIT_VM(n) asm volatile("s_waitcnt vmcnt(" #n ")" ::: "memory")
#define BARRIER() asm volatile("s_waitcnt lgkmcnt(0)\n\ts_barrier" ::: "memory")

__device__ __forceinline__ unsigned pk_bf16(float lo, float hi) {
  typedef __bf16 bf16x2 __attribute__((ext_vector_type(2)));
  f32x2 v = {lo, hi};
  bf16x2 r = __builtin_convertvector(v, bf16x2);
  return __builtin_bit_cast(unsigned, r);
}
__device__ __forceinline__ float bf_round(float x) { return (float)(__bf16)x; }
__device__ __forceinline__ float bf_lo(unsigned u) { return __uint_as_float(u << 16); }
__device__ __forceinline__ float bf_hi(unsigned u) { return __uint_as_float(u & 0xffff0000u); }

__device__ __forceinline__ void glds16(const void* sbase, unsigned voff, unsigned lds_base) {
  unsigned keep;
  asm volatile("s_mov_b32 %0, m0\n\ts_mov_b32 m0, %3\n\ts_nop 0\n\tglobal_load_lds_dwordx4 %1, %2\n\ts_mov_b32 m0, %0"
               : "=&s"(keep) : "v"(voff), "s"(sbase), "s"(lds_base) : "memory");
}
__device__ __forceinline__ s16x4 tr_read(const lds_char* p) {
  return __builtin_amdgcn_ds_read_tr16_b64_v4i16((__attribute__((address_space(3))) s16x4*)p);
}
__device__ __forceinline__ bf16x8 tr_frag(const lds_char* p0, const lds_char* p1) {
  s16x4 a = tr_read(p0), b = tr_read(p1);
  typedef short s16x8 __attribute__((ext_vector_type(8)));
  s16x8 r = __builtin_shufflevector(a, b, 0, 1, 2, 3, 4, 5, 6, 7);
  return __builtin_bit_cast(bf16x8, r);
}
__device__ __forceinline__ bf16x8 lds_frag(const lds_char* p) {
  return *(const __attribute__((address_space(3))) bf16x8*)p;
}
__device__ __forceinline__ u32x4 pack8(const f32x16& a, int o, float s) {
  u32x4 r;
  r[0] = pk_bf16(a[o + 0] * s, a[o + 1] * s);
  r[1] = pk_bf16(a[o + 2] * s, a[o + 3] * s);
  r[2] = pk_bf16(a[o + 4] * s, a[o + 5] * s);
  r[3] = pk_bf16(a[o + 6] * s, a[o + 7] * s);
  return r;
}
__device__ __forceinline__ float swap_add32(float v) {
  auto r = __builtin_amdgcn_permlane32_swap(__float_as_uint(v), __float_as_uint(v), false, false);
  return __uint_as_float(r[0]) + __uint_as_float(r[1]);
}
__device__ __forceinline__ float swap_max32(float v) {
  auto r = __builtin_amdgcn_permlane32_swap(__float_as_uint(v), __float_as_uint(v), false, false);
  return fmaxf(__uint_as_float(r[0]), __uint_as_float(r[1]));
}

constexpr int K1_XB = 65536, K1_STG = 24576, K1_LDS = K1_XB + 4 * K1_STG;
__global__ __launch_bounds__(512) void k_qkv(const uint16_t* __restrict__ x, const uint16_t* __restrict__ wqkv, char* __restrict__ ws) {
  extern __shared__ __attribute__((aligned(16))) char smem[];
  lds_char* lds = (lds_char*)smem;
  const unsigned lds0 = (unsigned)(uintptr_t)lds;
  const int tid = threadIdx.x, lane = tid & 63;
  const int w = __builtin_amdgcn_readfirstlane(tid >> 6);
  const int xcd = blockIdx.x & 7, idx = blockIdx.x >> 3;
  const int tb = (xcd >> 1) * 8 + (idx >> 2), cg = (xcd & 1) * 4 + (idx & 3);
  const int r8 = lane >> 3, c8 = lane & 7;
  const uint16_t* xrow = x + (size_t)(tb * 64 + w * 8 + r8) * DM + c8 * 8;
  u32x4 xv[8];
#pragma unroll
  for (int i = 0; i < 8; ++i) asm volatile("global_load_dwordx4 %0, %1, off offset:%c2" : "=v"(xv[i]) : "v"(xrow), "i"(i * 128) : "memory");
  const unsigned wsrc = (unsigned)((w * 8 + r8) * (3 * DM * 2) + cg * 128 + ((c8 ^ (((r8 >> 1) & 1) << 2)) * 16));
  auto stage_w = [&](int c) {
    const char* b = (const char*)wqkv + (size_t)c * 64 * (3 * DM * 2);
    const unsigned dst = lds0 + K1_XB + (c & 3) * K1_STG + w * 1024;
    glds16(b, wsrc, dst);
    glds16(b + 1024, wsrc, dst + 8192);
    glds16(b + 2048, wsrc, dst + 16384);
  };
  stage_w(0); stage_w(1); stage_w(2); stage_w(3);
  asm volatile("s_waitcnt vmcnt(12)" : "+v"(xv[0]), "+v"(xv[1]), "+v"(xv[2]), "+v"(xv[3]), "+v"(xv[4]), "+v"(xv[5]), "+v"(xv[6]), "+v"(xv[7]) :: "memory");
  float d[64];
  float sum = 0.f;
#pragma unroll
  for (int i = 0; i < 8; ++i)
#pragma unroll
    for (int j = 0; j < 4; ++j) { d[i * 8 + 2 * j] = bf_lo(xv[i][j]); d[i * 8 + 2 * j + 1] = bf_hi(xv[i][j]); sum += d[i * 8 + 2 * j] + d[i * 8 + 2 * j + 1]; }
  sum += __shfl_xor(sum, 1); sum += __shfl_xor(sum, 2); sum += __shfl_xor(sum, 4);
  const float mean = sum * (1.0f / DM), mb = bf_round(mean);
  float sq = 0.f;
#pragma unroll
  for (int i = 0; i < 64; ++i) { d[i] -= mb; sq = fmaf(d[i], d[i], sq); }
  sq += __shfl_xor(sq, 1); sq += __shfl_xor(sq, 2); sq += __shfl_xor(sq, 4);
  const float dm = mean - mb;
  const float var = fmaxf(sq * (1.0f / DM) - dm * dm, 0.f);
  const float sd = bf_round(sqrtf(bf_round(bf_round(var) + 1e-5f)));
  const float rs = 1.0f / sd;
  const int t = (w * 8 + r8) & 31, ttl = w >> 2;
#pragma unroll
  for (int i = 0; i < 8; ++i) {
    u32x4 o;
#pragma unroll
    for (int j = 0; j < 4; ++j) o[j] = pk_bf16(d[i * 8 + 2 * j] * rs, d[i * 8 + 2 * j + 1] * rs);
    const int ks = 4 * i + (c8 >> 1), h = c8 & 1;
    *(__attribute__((address_space(3))) u32x4*)(lds + ((ttl * 32 + ks) * 64 + h * 32 + (t ^ c8)) * 16) = o;
    if (i == cg) *(u32x4*)(ws + WS_XN + ((size_t)((tb * 2 + ttl) * 32 + ks) * 64 + h * 32 + t) * 16) = o;
  }
  const int cttl = (w >> 1) & 1, hp = w & 1;
  const int l31 = lane & 31, hh = lane >> 5, blk = (lane >> 4) & 1, q4 = (lane & 15) >> 2, p4 = lane & 3;
  const lds_char* xb = lds + (cttl * 32 * 64 + hh * 32) * 16;
  const lds_char* wb = lds + K1_XB + (8 * hh + q4) * 128 + ((64 * hp + 32 * blk + 8 * p4) ^ ((q4 >> 1) << 6));
  f32x16 aq = {}, ak = {}, av = {};
#pragma unroll
  for (int c = 0; c < 8; ++c) {
    if (c == 0) { WAIT_VM(9); } else if (c < 5) { WAIT_VM(6); } else if (c == 5) { WAIT_VM(6); } else if (c == 6) { WAIT_VM(3); } else { WAIT_VM(0); }
    BARRIER();
    if (c >= 1 && c + 3 < 8) stage_w(c + 3);
    if (w < 4) {
#pragma unroll
      for (int k4 = 0; k4 < 4; ++k4) {
        const int ks = c * 4 + k4;
        bf16x8 xf = lds_frag(xb + ks * 1024 + ((l31 ^ ((2 * ks + hh) & 7)) * 16));
        const lds_char* wp = wb + (c & 3) * K1_STG + k4 * 2048;
        bf16x8 wq = tr_frag(wp, wp + 512), wk = tr_frag(wp + 8192, wp + 8192 + 512), wv = tr_frag(wp + 16384, wp + 16384 + 512);
        aq = MFMA32(wq, xf, aq);
        ak = MFMA32(wk, xf, ak);
        av = MFMA32(xf, wv, av);
      }
    }
  }
  if (w < 4) {
    const int tt = tb * 2 + cttl, h0 = cg * 4 + hp * 2;
    const float qs = 0.25f * 1.4426950408889634f;
    char* qf = ws + WS_Q + ((size_t)h0 * 64 + tt) * 1024 + lane * 16;
    *(u32x4*)qf = pack8(aq, 0, qs);
    *(u32x4*)(qf + 65536) = pack8(aq, 8, qs);
    const int sl = (l31 & 0x13) | ((l31 & 4) << 1) | ((l31 & 8) >> 1);
    char* kv = ws + WS_KV + ((size_t)h0 * 64 + tt) * 2048;
    *(u32x4*)(kv + (hh * 32 + sl) * 16) = pack8(ak, 0, 1.f);
    *(u32x4*)(kv + 131072 + (hh * 32 + sl) * 16) = pack8(ak, 8, 1.f);
    char* vp = kv + (size_t)blk * 131072 + 1024 + hh * 256 + (lane & 15) * 16;
    *(u32x4*)vp = pack8(av, 0, 1.f);
    *(u32x4*)(vp + 512) = pack8(av, 8, 1.f);
    float n0 = 0.f, n1 = 0.f;
#pragma unroll
    for (int r = 0; r < 8; ++r) { n0 = fmaf(ak[r], ak[r], n0); n1 = fmaf(ak[8 + r], ak[8 + r], n1); }
    n0 = swap_add32(n0); n1 = swap_add32(n1);
    unsigned u0 = __float_as_uint(n0) & 0x7fffffffu, u1 = __float_as_uint(n1) & 0x7fffffffu;
#pragma unroll
    for (int o = 1; o < 32; o <<= 1) { u0 = max(u0, (unsigned)__shfl_xor((int)u0, o)); u1 = max(u1, (unsigned)__shfl_xor((int)u1, o)); }
    if (lane == 0) {
      ((unsigned*)(ws + WS_KMAX))[h0 * 64 + tt] = u0;
      ((unsigned*)(ws + WS_KMAX))[(h0 + 1) * 64 + tt] = u1;
    }
  }
}

constexpr int F1_UP = 14336 + 128, F1_X = F1_UP + 14336, F1_STG = F1_X + 16384, F1_LDS = 3 * F1_STG;
__device__ __forceinline__ float gelu_mul(float g, float u) {
  const float c0 = -2.0f * 0.7978845608028654f * 1.4426950408889634f, c1 = c0 * 0.044715f;
  float z = g * fmaf(g * g, c1, c0);
  float e = __builtin_amdgcn_exp2f(z);
  return g * __builtin_amdgcn_rcpf(1.0f + e) * u;
}
__global__ __launch_bounds__(256) void k_ffn1(const uint16_t* __restrict__ wg, const uint16_t* __restrict__ wu, char* __restrict__ ws) {
  extern __shared__ __attribute__((aligned(16))) char smem[];
  lds_char* lds = (lds_char*)smem;
  const unsigned lds0 = (unsigned)(uintptr_t)lds;
  const int tid = threadIdx.x, lane = tid & 63;
  const int w = __builtin_amdgcn_readfirstlane(tid >> 6);
  const int xcd = blockIdx.x & 7, idx = blockIdx.x >> 3;
  const int tb = (xcd & 1) * 8 + (idx >> 2), fg = (xcd >> 1) * 4 + (idx & 3);
  const int tt = tb * 4 + w;
  unsigned wsrc[7];
#pragma unroll
  for (int e = 0; e < 7; ++e) { const int ci = ((w & 1) * 7 + e) * 64 + lane; wsrc[e] = (ci / 14) * (FF * 2) + (ci % 14) * 16; }
  const char* wbase = (const char*)(w < 2 ? wg : wu) + fg * 224;
  const char* xbase = ws + WS_XN + (size_t)tt * 32768;
  const unsigned l16 = lane * 16;
  auto stage = [&](int c) {
    const unsigned s = lds0 + (c % 3) * F1_STG;
    const char* b = wbase + (size_t)c * 64 * (FF * 2);
    const unsigned dst = s + (w < 2 ? 0 : F1_UP) + (w & 1) * 7168;
#pragma unroll
    for (int e = 0; e < 7; ++e) glds16(b, wsrc[e], dst + e * 1024);
#pragma unroll
    for (int k4 = 0; k4 < 4; ++k4) glds16(xbase + (c * 4 + k4) * 1024, l16, s + F1_X + w * 4096 + k4 * 1024);
  };
  stage(0); stage(1);
  const int hh = lane >> 5, blk = (lane >> 4) & 1, q4 = (lane & 15) >> 2, p4 = lane & 3;
  const lds_char* wb = lds + blk * F1_UP + (8 * hh + q4) * 224 + 8 * p4;
  const lds_char* xb = lds + F1_X + w * 4096 + lane * 16;
  f32x16 acc[7];
#pragma unroll
  for (int j = 0; j < 7; ++j) acc[j] = f32x16{};
#pragma unroll
  for (int c = 0; c < 8; ++c) {
    if (c < 7) { WAIT_VM(11); } else { WAIT_VM(0); }
    BARRIER();
    if (c + 2 < 8) stage(c + 2);
#pragma unroll
    for (int k4 = 0; k4 < 4; ++k4) {
      bf16x8 xf = lds_frag(xb + (c % 3) * F1_STG + k4 * 1024);
      const lds_char* wp = wb + (c % 3) * F1_STG + k4 * 16 * 224;
#pragma unroll
      for (int j = 0; j < 7; ++j) acc[j] = MFMA32(tr_frag(wp + j * 32, wp + j * 32 + 4 * 224), xf, acc[j]);
    }
  }
  char* hp = ws + WS_H + ((size_t)tt * 112 + fg * 7) * 1024 + lane * 16;
#pragma unroll
  for (int j = 0; j < 7; ++j) {
    u32x4 o;
#pragma unroll
    for (int r = 0; r < 4; ++r) o[r] = pk_bf16(gelu_mul(acc[j][2 * r], acc[j][8 + 2 * r]), gelu_mul(acc[j][2 * r + 1], acc[j][9 + 2 * r]));
    *(u32x4*)(hp + j * 1024) = o;
  }
}

__device__ __forceinline__ f32x16 pv_lo(const bf16x8& v, const f32x16& p, const f32x16& o, int idx) {
  f32x8 h = __builtin_shufflevector(p, p, 0, 1, 2, 3, 4, 5, 6, 7);
  return __builtin_amdgcn_smfmac_f32_32x32x32_bf16(v, __builtin_bit_cast(bf16x16, h), o, idx, 0, 0);
}
__device__ __forceinline__ f32x16 pv_hi(const bf16x8& v, const f32x16& p, const f32x16& o, int idx) {
  f32x8 h = __builtin_shufflevector(p, p, 8, 9, 10, 11, 12, 13, 14, 15);
  return __builtin_amdgcn_smfmac_f32_32x32x32_bf16(v, __builtin_bit_cast(bf16x16, h), o, idx, 0, 0);
}
#define EXP(S, i) S[i] = __builtin_amdgcn_exp2f(S[i])
constexpr int AT_LDS = 8 * 4 * 2560;
__global__ __launch_bounds__(512) void k_attn(char* __restrict__ ws) {
  extern __shared__ __attribute__((aligned(16))) char smem[];
  lds_char* lds = (lds_char*)smem;
  const int tid = threadIdx.x, lane = tid & 63;
  const int w = __builtin_amdgcn_readfirstlane(tid >> 6);
  const int head = (blockIdx.x & 7) * 4 + ((blockIdx.x >> 3) & 3), qb = blockIdx.x >> 5;
  const int g = w >> 2, kq = w & 3;
  const char* kvb = ws + WS_KV + ((size_t)head * 64 + kq * 16) * 2048;
  const unsigned koff = lane * 16, voff = (lane >> 5) * 256 + (lane & 15) * 16;
  const unsigned long long vmask = 0x0000ffff0000ffffull;
  bf16x8 kA, kB, vA0, vA1, vB0, vB1;
  {
    u32x4 ones = {0x3f803f80u, 0x3f803f80u, 0x3f803f80u, 0x3f803f80u};
    vA0 = vA1 = vB0 = vB1 = __builtin_bit_cast(bf16x8, ones);
  }
#define LOADK(dst, t) asm volatile("global_load_dwordx4 %0, %1, %2" : "=v"(dst) : "v"(koff), "s"(kvb + (t) * 2048) : "memory")
#define LOADV(d0, d1, t) asm volatile("s_mov_b64 exec, %4\n\tglobal_load_dwordx4 %0, %2, %3 offset:1024\n\tglobal_load_dwordx4 %1, %2, %3 offset:1536\n\ts_mov_b64 exec, -1" \
                                      : "+v"(d0), "+v"(d1) : "v"(voff), "s"(kvb + (t) * 2048), "s"(vmask) : "memory")
  LOADK(kA, 0); LOADV(vA0, vA1, 0); LOADK(kB, 1);
  bf16x8 qf[4];
  const char* qp = ws + WS_Q + ((size_t)head * 64 + qb * 8 + g * 4) * 1024 + lane * 16;
#pragma unroll
  for (int i = 0; i < 4; ++i) qf[i] = *(const bf16x8*)(qp + i * 1024);
  unsigned km = ((const unsigned*)(ws + WS_KMAX))[head * 64 + lane];
#pragma unroll
  for (int o = 1; o < 64; o <<= 1) km = max(km, (unsigned)__shfl_xor((int)km, o));
  float qn = 0.f;
#pragma unroll
  for (int i = 0; i < 4; ++i) {
    float s = 0.f;
#pragma unroll
    for (int j = 0; j < 8; ++j) { float v = (float)qf[i][j]; s = fmaf(v, v, s); }
    s = swap_add32(s);
    qn = (s > qn || s != s) ? s : qn;
  }
  const float bound = sqrtf(qn * __uint_as_float(km)) * 1.02f;
  const bool fast = __all(bound <= 40.0f);
  const int sidx = 0xdddd;
  f32x16 o[4];
  float m[4];
#pragma unroll
  for (int i = 0; i < 4; ++i) o[i] = f32x16{};
  if (fast) {
    f32x16 negm;
#pragma unroll
    for (int r = 0; r < 16; ++r) negm[r] = -bound;
    asm volatile("" : "+v"(negm));
    f32x16 sa, sb = f32x16{};
    asm volatile("s_waitcnt vmcnt(0)" : "+v"(kA), "+v"(kB), "+v"(vA0), "+v"(vA1) :: "memory");
    sa = MFMA32(kA, qf[0], negm);
#define BODY(C, N, OPREV, VPREV1, OCUR, VCUR0, KNEXT, QNEXT, PRE_A, PRE_B, PRE_C)                        \
    SBAR(); EXP(C, 0); EXP(C, 1); SBAR();                                                                 \
    OPREV = pv_hi(VPREV1, N, OPREV, sidx); SBAR(); PRE_A;                                                 \
    EXP(C, 2); EXP(C, 3); EXP(C, 4); EXP(C, 5); SBAR(); PRE_B;                                            \
    N = MFMA32(KNEXT, QNEXT, negm); SBAR();                                                               \
    EXP(C, 6); EXP(C, 7); EXP(C, 8); EXP(C, 9); EXP(C, 10); SBAR(); PRE_C;                                \
    OCUR = pv_lo(VCUR0, C, OCUR, sidx); SBAR();                                                           \
    EXP(C, 11); EXP(C, 12); EXP(C, 13); EXP(C, 14); EXP(C, 15); SBAR();
#define WAITV(a, b) asm volatile("s_waitcnt vmcnt(3)" : "+v"(a), "+v"(b) :: "memory")
#define WAITK(a) asm volatile("s_waitcnt vmcnt(3)" : "+v"(a) :: "memory")
#define TILE(KC, KN, VC0, VC1, VN0, VN1, t)                                                               \
    BODY(sa, sb, o[3], VN1, o[0], VC0, KC, qf[1], LOADV(VN0, VN1, (t) + 1), , WAITV(VC0, VC1))            \
    BODY(sb, sa, o[0], VC1, o[1], VC0, KC, qf[2], , , )                                                   \
    BODY(sa, sb, o[1], VC1, o[2], VC0, KC, qf[3], , , LOADK(KC, (t) + 2))                                 \
    BODY(sb, sa, o[2], VC1, o[3], VC0, KN, qf[0], , WAITK(KN), )
    for (int t = 0; t < 16; t += 2) {
      TILE(kA, kB, vA0, vA1, vB0, vB1, t)
      TILE(kB, kA, vB0, vB1, vA0, vA1, t + 1)
    }
    o[3] = pv_hi(vB1, sb, o[3], sidx);
    asm volatile("s_waitcnt vmcnt(0)" : "+v"(kA), "+v"(kB), "+v"(vA0), "+v"(vA1) :: "memory");
#pragma unroll
    for (int i = 0; i < 4; ++i) m[i] = bound;
  } else {
    asm volatile("s_waitcnt vmcnt(0)" : "+v"(kA), "+v"(kB), "+v"(vA0), "+v"(vA1) :: "memory");
#pragma unroll
    for (int i = 0; i < 4; ++i) m[i] = -INFINITY;
    for (int t = 0; t < 16; ++t) {
      LOADK(kA, t); LOADV(vA0, vA1, t);
      asm volatile("s_waitcnt vmcnt(0)" : "+v"(kA), "+v"(vA0), "+v"(vA1) :: "memory");
#pragma unroll
      for (int i = 0; i < 4; ++i) {
        f32x16 s = MFMA32(kA, qf[i], f32x16{});
        float mx = s[0];
#pragma unroll
        for (int r = 1; r < 16; ++r) mx = fmaxf(mx, s[r]);
        mx = fmaxf(swap_max32(mx), m[i]);
        const float ms = (mx == -INFINITY) ? 0.f : mx;
        const float al = __builtin_amdgcn_exp2f(m[i] - ms);
        m[i] = mx;
#pragma unroll
        for (int r = 0; r < 16; ++r) { s[r] = __builtin_amdgcn_exp2f(s[r] - ms); o[i][r] *= al; }
        o[i] = pv_lo(vA0, s, o[i], sidx);
        o[i] = pv_hi(vA1, s, o[i], sidx);
      }
    }
  }
#pragma unroll
  for (int i = 0; i < 4; ++i) {
    lds_char* p = lds + (w * 4 + i) * 2560;
    *(__attribute__((address_space(3))) f32x4*)(p + lane * 16) = f32x4{o[i][0], o[i][1], o[i][2], o[i][3]};
    *(__attribute__((address_space(3))) f32x4*)(p + 1024 + lane * 16) = f32x4{o[i][4], o[i][5], o[i][6], o[i][7]};
    *(__attribute__((address_space(3))) f32x2*)(p + 2048 + lane * 8) = f32x2{o[i][8], m[i]};
  }
  __syncthreads();
  {
    f32x4 a[4], b[4]; f32x2 c[4];
#pragma unroll
    for (int k = 0; k < 4; ++k) {
      const lds_char* p = lds + ((g * 4 + k) * 4 + kq) * 2560;
      a[k] = *(const __attribute__((address_space(3))) f32x4*)(p + lane * 16);
      b[k] = *(const __attribute__((address_space(3))) f32x4*)(p + 1024 + lane * 16);
      c[k] = *(const __attribute__((address_space(3))) f32x2*)(p + 2048 + lane * 8);
    }
    const float M = fmaxf(fmaxf(c[0][1], c[1][1]), fmaxf(c[2][1], c[3][1]));
    f32x4 sa4 = {}, sb4 = {}; float l = 0.f;
#pragma unroll
    for (int k = 0; k < 4; ++k) { const float wk = __builtin_amdgcn_exp2f(c[k][1] - M); sa4 += a[k] * wk; sb4 += b[k] * wk; l += c[k][0] * wk; }
    const float rl = 1.0f / l;
    u32x4 r = {pk_bf16(sa4[0] * rl, sa4[1] * rl), pk_bf16(sa4[2] * rl, sa4[3] * rl), pk_bf16(sb4[0] * rl, sb4[1] * rl), pk_bf16(sb4[2] * rl, sb4[3] * rl)};
    *(u32x4*)(ws + WS_O + ((size_t)(qb * 8 + g * 4 + kq) * 32 + head) * 1024 + lane * 16) = r;
  }
}

constexpr int K3_DEPTH = 8, K3_LDS = 4 * K3_DEPTH * 4096;
__global__ __launch_bounds__(256) void k_out(const uint16_t* __restrict__ x, const uint16_t* __restrict__ wo, const uint16_t* __restrict__ wd,
                                             const char* __restrict__ ws, uint16_t* __restrict__ out) {
  extern __shared__ __attribute__((aligned(16))) char smem[];
  lds_char* lds = (lds_char*)smem;
  const unsigned lds0 = (unsigned)(uintptr_t)lds;
  const int tid = threadIdx.x, lane = tid & 63;
  const int w = __builtin_amdgcn_readfirstlane(tid >> 6);
  const int xcd = blockIdx.x & 7, idx = blockIdx.x >> 3;
  const int tb = (xcd >> 1) * 8 + (idx >> 2), cb = (xcd & 1) * 4 + (idx & 3);
  const int r8 = lane >> 3, c8 = lane & 7;
  const unsigned wsrc = r8 * (DM * 2) + cb * 128 + ((c8 ^ (((r8 >> 1) & 1) << 2)) * 16);
  const unsigned l16 = lane * 16;
  const unsigned ring = lds0 + w * (K3_DEPTH * 4096);
  auto stage = [&](int s) {
    const char *wsb, *a0; size_t astr;
    if (s < 28) { const int fs = w * 28 + s; wsb = (const char*)wd + (size_t)fs * 16 * (DM * 2); a0 = ws + WS_H + ((size_t)tb * 2 * 112 + fs) * 1024; astr = 112 * 1024; }
    else { const int hd = w * 8 + (s - 28); wsb = (const char*)wo + (size_t)hd * 16 * (DM * 2); a0 = ws + WS_O + ((size_t)tb * 2 * 32 + hd) * 1024; astr = 32 * 1024; }
    const unsigned dst = ring + (s % K3_DEPTH) * 4096;
    glds16(wsb, wsrc, dst);
    glds16(wsb + 8 * DM * 2, wsrc, dst + 1024);
    glds16(a0, l16, dst + 2048);
    glds16(a0 + astr, l16, dst + 3072);
  };
#pragma unroll
  for (int s = 0; s < K3_DEPTH - 1; ++s) stage(s);
  const int hh = lane >> 5, blk = (lane >> 4) & 1, q4 = (lane & 15) >> 2, p4 = lane & 3;
  const lds_char* wb0 = lds + w * (K3_DEPTH * 4096) + (4 * hh + q4) * 128 + ((32 * blk + 8 * p4) ^ ((q4 >> 1) << 6));
  const lds_char* wb1 = lds + w * (K3_DEPTH * 4096) + (4 * hh + q4) * 128 + ((64 + 32 * blk + 8 * p4) ^ ((q4 >> 1) << 6));
  const lds_char* ab = lds + w * (K3_DEPTH * 4096) + 2048 + lane * 16;
  f32x16 acc[2][2];
#pragma unroll
  for (int i = 0; i < 2; ++i) for (int j = 0; j < 2; ++j) acc[i][j] = f32x16{};
#pragma unroll
  for (int s = 0; s < 36; ++s) {
    if (s + K3_DEPTH - 1 < 36) { asm volatile("s_waitcnt lgkmcnt(0)" ::: "memory"); stage(s + K3_DEPTH - 1); WAIT_VM(28); }
    else {
      switch (35 - s) { case 6: WAIT_VM(24); break; case 5: WAIT_VM(20); break; case 4: WAIT_VM(16); break; case 3: WAIT_VM(12); break; case 2: WAIT_VM(8); break; case 1: WAIT_VM(4); break; default: WAIT_VM(0); }
    }
    const int so = (s % K3_DEPTH) * 4096;
    bf16x8 w0 = tr_frag(wb0 + so, wb0 + so + 1024), w1 = tr_frag(wb1 + so, wb1 + so + 1024);
    bf16x8 a0 = lds_frag(ab + so), a1 = lds_frag(ab + so + 1024);
    acc[0][0] = MFMA32(w0, a0, acc[0][0]);
    acc[0][1] = MFMA32(w1, a0, acc[0][1]);
    acc[1][0] = MFMA32(w0, a1, acc[1][0]);
    acc[1][1] = MFMA32(w1, a1, acc[1][1]);
  }
  __syncthreads();
  constexpr int RS = 272;
#pragma unroll
  for (int i = 0; i < 2; ++i)
#pragma unroll
    for (int j = 0; j < 2; ++j)
#pragma unroll
      for (int r4 = 0; r4 < 4; ++r4) {
        const int tok = i * 32 + (lane & 31), n = j * 32 + 8 * r4 + 4 * hh;
        *(__attribute__((address_space(3))) f32x4*)(lds + w * (64 * RS) + tok * RS + n * 4) =
            f32x4{acc[i][j][4 * r4], acc[i][j][4 * r4 + 1], acc[i][j][4 * r4 + 2], acc[i][j][4 * r4 + 3]};
      }
  __syncthreads();
#pragma unroll
  for (int it = 0; it < 2; ++it) {
    const int e = it * 256 + tid, tok = e >> 3, c = e & 7;
    f32x4 s0 = {}, s1 = {};
#pragma unroll
    for (int k = 0; k < 4; ++k) {
      s0 += *(const __attribute__((address_space(3))) f32x4*)(lds + k * (64 * RS) + tok * RS + c * 32);
      s1 += *(const __attribute__((address_space(3))) f32x4*)(lds + k * (64 * RS) + tok * RS + c * 32 + 16);
    }
    const size_t go = (size_t)(tb * 64 + tok) * DM + cb * 64 + c * 8;
    const u32x4 xv = *(const u32x4*)(x + go);
    u32x4 r = {pk_bf16(s0[0] + bf_lo(xv[0]), s0[1] + bf_hi(xv[0])), pk_bf16(s0[2] + bf_lo(xv[1]), s0[3] + bf_hi(xv[1])),
               pk_bf16(s1[0] + bf_lo(xv[2]), s1[1] + bf_hi(xv[2])), pk_bf16(s1[2] + bf_lo(xv[3]), s1[3] + bf_hi(xv[3]))};
    *(u32x4*)(out + go) = r;
  }
}

extern "C" void kernel_launch(void* const* d_in, const int* in_sizes, int n_in, void* d_out, int out_size, void* d_ws, size_t ws_size,
                              hipStream_t stream) {
  const uint16_t* x = (const uint16_t*)d_in[0];
  const uint16_t* wqkv = (const uint16_t*)d_in[1];
  const uint16_t* wo = (const uint16_t*)d_in[2];
  const uint16_t* wg = (const uint16_t*)d_in[4];
  const uint16_t* wu = (const uint16_t*)d_in[5];
  const uint16_t* wd = (const uint16_t*)d_in[6];
  char* ws = (char*)d_ws;
  (void)hipFuncSetAttribute((const void*)k_qkv, hipFuncAttributeMaxDynamicSharedMemorySize, K1_LDS);
  (void)hipFuncSetAttribute((const void*)k_ffn1, hipFuncAttributeMaxDynamicSharedMemorySize, F1_LDS);
  (void)hipFuncSetAttribute((const void*)k_attn, hipFuncAttributeMaxDynamicSharedMemorySize, AT_LDS);
  (void)hipFuncSetAttribute((const void*)k_out, hipFuncAttributeMaxDynamicSharedMemorySize, K3_LDS);
  k_qkv<<<256, 512, K1_LDS, stream>>>(x, wqkv, ws);
  k_ffn1<<<256, 256, F1_LDS, stream>>>(wg, wu, ws);
  k_attn<<<256, 512, AT_LDS, stream>>>(ws);
  k_out<<<256, 256, K3_LDS, stream>>>(x, wo, wd, ws, (uint16_t*)d_out);
}
```

```cpp
#include <hip/hip_runtime.h>
#include <hip/hip_bf16.h>
#include <stdint.h>

typedef __bf16 bf16x2 __attribute__((ext_vector_type(2)));
typedef __bf16 bf16x8 __attribute__((ext_vector_type(8)));
typedef __bf16 bf16x16 __attribute__((ext_vector_type(16)));
typedef float f32x16 __attribute__((ext_vector_type(16)));
typedef float f32x8 __attribute__((ext_vector_type(8)));
typedef float f32x4 __attribute__((ext_vector_type(4)));
typedef float f32x2 __attribute__((ext_vector_type(2)));
typedef unsigned u32x4 __attribute__((ext_vector_type(4)));
typedef short s16x4 __attribute__((ext_vector_type(4)));
typedef short s16x8 __attribute__((ext_vector_type(8)));
typedef unsigned long long u64;
#define LAS __attribute__((address_space(3)))
#define GAS __attribute__((address_space(1)))
typedef LAS char lds_char;

constexpr int DM = 512, FF = 1792;
constexpr size_t WS_EPOCH = 0;
constexpr int FS = 128;
constexpr size_t WS_FQKV = 65536;
constexpr size_t WS_FH = WS_FQKV + 256 * FS;
constexpr size_t WS_FXN = WS_FH + 256 * FS;
constexpr size_t WS_FO = WS_FXN + 256 * FS;
constexpr size_t WS_Q = 2u << 20;
constexpr size_t WS_KV = 4u << 20;
constexpr size_t WS_O = 10u << 20;
constexpr size_t WS_H = 12u << 20;
constexpr size_t WS_XN = 20u << 20;
constexpr int L_RING = 65536, QSTG = 16384;
constexpr int A_REG = 10240;
constexpr int L_CTL = 81920;
constexpr int L_PRG = L_CTL + 256;
constexpr int L_KVL = L_PRG + 1024;
constexpr int L_F = 90112, F1_CH = 2816, F1_STG = 4 * F1_CH;
constexpr int LDS_BYTES = 163840;
enum { C_PUB = 0, C_HPUB = 2, C_CMB0 = 3, C_OPUB = 5, C_KVOK = 6, C_HPUB2 = 7, C_KM = 8, C_KQ = 16 };


#define EPOCH_OUT "={s[98:99]}"
#define EPOCH_IO "+{s[98:99]}"
#define SBAR() __builtin_amdgcn_sched_barrier(0)
#define MFMA32(a, b, c) __builtin_amdgcn_mfma_f32_32x32x16_bf16(a, b, c, 0, 0, 0)
#define WAIT_VM(n) asm volatile("s_waitcnt vmcnt(" #n ")" ::: "memory")
#define BARRIER() asm volatile("s_waitcnt lgkmcnt(0)\n\ts_barrier" ::: "memory")

__device__ __forceinline__ unsigned pk_bf16(float lo, float hi) {
  f32x2 v = {lo, hi};
  return __builtin_bit_cast(unsigned, __builtin_convertvector(v, bf16x2));
}
__device__ __forceinline__ bf16x2 as_bf2(unsigned u) { return __builtin_bit_cast(bf16x2, u); }
__device__ __forceinline__ float bf_round(float x) { return (float)(__bf16)x; }
__device__ __forceinline__ float bf_lo(unsigned u) { return __uint_as_float(u << 16); }
__device__ __forceinline__ float bf_hi(unsigned u) { return __uint_as_float(u & 0xffff0000u); }

template <int K> __device__ __forceinline__ const void* ssel(int f, const void* a, const void* b) {
  const void* r;
  asm("s_cmp_eq_u32 %1, %c4\n\ts_cselect_b64 %0, %2, %3" : "=s"(r) : "s"(f), "s"(a), "s"(b), "i"(K) : "scc");
  return r;
}
__device__ __forceinline__ u32x4 make_srd(const void* p, unsigned bytes) {
  const u64 a = (u64)p;
  return u32x4{(unsigned)a, (unsigned)(a >> 32) & 0xffffu, bytes, 0x00020000u};
}
__device__ __forceinline__ void bdma2(u32x4 srd, unsigned so, unsigned va, unsigned vb, unsigned dst) {
  unsigned keep;
  asm volatile("s_mov_b32 %0, m0\n\ts_mov_b32 m0, %5\n\ts_nop 0\n\tbuffer_load_dwordx4 %1, %3, %4 offen lds\n\t"
               "buffer_load_dwordx4 %2, %3, %4 offen offset:1024 lds\n\ts_mov_b32 m0, %0"
               : "=&s"(keep) : "v"(va), "v"(vb), "s"(srd), "s"(so), "s"(dst) : "memory");
}
__device__ __forceinline__ void bdma_f1w(u32x4 rg, u32x4 ru, unsigned so, unsigned va, unsigned dst) {
  unsigned keep;
  asm volatile("s_mov_b32 %0, m0\n\ts_mov_b32 m0, %5\n\ts_mov_b64 exec, %6\n\t"
               "buffer_load_dwordx4 %1, %2, %4 offen lds\n\tbuffer_load_dwordx4 %1, %3, %4 offen offset:896 lds\n\t"
               "s_mov_b64 exec, -1\n\ts_mov_b32 m0, %0"
               : "=&s"(keep) : "v"(va), "s"(rg), "s"(ru), "s"(so), "s"(dst), "s"(0x00ffffffffffffffull) : "memory");
}
__device__ __forceinline__ void bdma_f1x(u32x4 rx, unsigned so, unsigned vx, unsigned dst) {
  unsigned keep;
  asm volatile("s_mov_b32 %0, m0\n\ts_mov_b32 m0, %4\n\ts_nop 0\n\tbuffer_load_dwordx4 %1, %2, %3 offen offset:1792 sc1 lds\n\ts_mov_b32 m0, %0"
               : "=&s"(keep) : "v"(vx), "s"(rx), "s"(so), "s"(dst) : "memory");
}
__device__ __forceinline__ void st16_dev(void* sbase, unsigned voff, u32x4 v) {
  asm volatile("global_store_dwordx4 %0, %1, %2 sc1\n\ts_nop 1" :: "v"(voff), "v"(v), "s"(sbase) : "memory");
}
__device__ __forceinline__ bf16x8 tr_frag(const lds_char* p0, const lds_char* p1) {
  s16x4 a = __builtin_amdgcn_ds_read_tr16_b64_v4i16((LAS s16x4*)p0), b = __builtin_amdgcn_ds_read_tr16_b64_v4i16((LAS s16x4*)p1);
  s16x8 r = __builtin_shufflevector(a, b, 0, 1, 2, 3, 4, 5, 6, 7);
  return __builtin_bit_cast(bf16x8, r);
}
__device__ __forceinline__ bf16x8 lds_frag(const lds_char* p) { return *(const LAS bf16x8*)p; }
__device__ __forceinline__ u32x4 pack8(const f32x16& a, int o, float s) {
  u32x4 r;
  r[0] = pk_bf16(a[o + 0] * s, a[o + 1] * s);
  r[1] = pk_bf16(a[o + 2] * s, a[o + 3] * s);
  r[2] = pk_bf16(a[o + 4] * s, a[o + 5] * s);
  r[3] = pk_bf16(a[o + 6] * s, a[o + 7] * s);
  return r;
}
__device__ __forceinline__ float swap_max32(float v) {
  auto r = __builtin_amdgcn_permlane32_swap(__float_as_uint(v), __float_as_uint(v), false, false);
  return fmaxf(__uint_as_float(r[0]), __uint_as_float(r[1]));
}
__device__ __forceinline__ int fresh_lane() {
  int l;
  asm volatile("v_mbcnt_lo_u32_b32 %0, -1, 0\n\tv_mbcnt_hi_u32_b32 %0, -1, %0" : "=v"(l));
  return l;
}
template <int CTRL> __device__ __forceinline__ float dpp_f(float v) {
  return __int_as_float(__builtin_amdgcn_update_dpp(0, __float_as_int(v), CTRL, 0xf, 0xf, true));
}
template <int CTRL> __device__ __forceinline__ unsigned dpp_u(unsigned v) {
  return (unsigned)__builtin_amdgcn_update_dpp(0, (int)v, CTRL, 0xf, 0xf, true);
}
__device__ __forceinline__ float sum8(float v) {
  v += dpp_f<0xB1>(v); v += dpp_f<0x4E>(v); v += dpp_f<0x141>(v); return v;
}
__device__ __forceinline__ unsigned umax16(unsigned v) {
  v = max(v, dpp_u<0xB1>(v)); v = max(v, dpp_u<0x4E>(v)); v = max(v, dpp_u<0x141>(v)); v = max(v, dpp_u<0x140>(v)); return v;
}

constexpr unsigned SPIN_G = 1u << 16, SPIN_L = 1u << 21;
__device__ __forceinline__ void wait_flags(const char* p, bool need, u64 tag) {
#pragma nounroll
  for (unsigned s = 0; s < SPIN_G; ++s) {
    bool ok = true;
    if (need) ok = __hip_atomic_load((const GAS u64*)p, __ATOMIC_RELAXED, __HIP_MEMORY_SCOPE_AGENT) == tag;
    if (__all(ok)) break;
    __builtin_amdgcn_s_sleep(1);
  }
  asm volatile("" ::: "memory");
}
template <int SLEEP>
__device__ __forceinline__ void lds_wait_t(lds_char* lds, int c, unsigned target) {
  unsigned v, s, bud = SPIN_L;
  asm volatile("1:\n\tds_read_b32 %0, %3\n\ts_waitcnt lgkmcnt(0)\n\tv_readfirstlane_b32 %1, %0\n\ts_nop 0\n\ts_cmp_ge_u32 %1, %4\n\ts_cbranch_scc1 2f\n\t"
               "s_sleep %c5\n\ts_sub_u32 %2, %2, 1\n\ts_cbranch_scc0 1b\n\t2:"
               : "=&v"(v), "=&s"(s), "+s"(bud) : "v"((unsigned)(uintptr_t)lds + L_CTL + c * 4), "s"(target), "i"(SLEEP) : "memory", "scc");
}
__device__ __forceinline__ void lds_wait(lds_char* lds, int c, unsigned target) { lds_wait_t<1>(lds, c, target); }
__device__ __forceinline__ void prg_wait(lds_char* lds, int lane, unsigned target) {
#pragma nounroll
  for (unsigned s = 0; s < SPIN_L; ++s) {
    unsigned v = __hip_atomic_load((LAS unsigned*)(lds + L_PRG + (lane & 3) * 256), __ATOMIC_RELAXED, __HIP_MEMORY_SCOPE_WORKGROUP);
    if (__all(v >= target)) break;
    __builtin_amdgcn_s_sleep(1);
  }
  asm volatile("" ::: "memory");
}
__device__ __forceinline__ void lds_arrive(lds_char* lds, int c, int) {
  asm volatile("s_waitcnt lgkmcnt(0)\n\ts_mov_b64 exec, 1\n\tds_add_u32 %0, %1\n\ts_mov_b64 exec, -1" :: "v"((unsigned)(uintptr_t)lds + L_CTL + c * 4), "v"(1u) : "memory");
}
__device__ __forceinline__ unsigned lds_ticket(lds_char* lds, int c) {
  unsigned v, s;
  asm volatile("s_mov_b64 exec, 1\n\tds_add_rtn_u32 %0, %2, %3\n\ts_mov_b64 exec, -1\n\ts_waitcnt lgkmcnt(0)\n\tv_readfirstlane_b32 %1, %0\n\ts_nop 1"
               : "=&v"(v), "=s"(s) : "v"((unsigned)(uintptr_t)lds + L_CTL + c * 4), "v"(1u) : "memory");
  return s;
}
__device__ __forceinline__ void count_and_flag(lds_char* lds, int c, unsigned n, char* flag, u64 tag, int) {
  if (lds_ticket(lds, c) == n - 1)
    asm volatile("s_mov_b64 exec, 0xffff\n\tglobal_store_dwordx2 %0, %1, %2 sc1\n\ts_mov_b64 exec, -1" :: "v"(fresh_lane() * 8u), "v"(tag), "s"(flag) : "memory");
}
__device__ __forceinline__ void publish(lds_char* lds, int c, unsigned n, char* flag, u64 tag, int lane) {
  asm volatile("s_waitcnt vmcnt(0)" ::: "memory");
  count_and_flag(lds, c, n, flag, tag, lane);
}

__device__ __forceinline__ f32x16 pv_lo(const bf16x8& v, const f32x16& p, const f32x16& o, int idx) {
  f32x8 h = __builtin_shufflevector(p, p, 0, 1, 2, 3, 4, 5, 6, 7);
  return __builtin_amdgcn_smfmac_f32_32x32x32_bf16(v, __builtin_bit_cast(bf16x16, h), o, idx, 0, 0);
}
__device__ __forceinline__ f32x16 pv_hi(const bf16x8& v, const f32x16& p, const f32x16& o, int idx) {
  f32x8 h = __builtin_shufflevector(p, p, 8, 9, 10, 11, 12, 13, 14, 15);
  return __builtin_amdgcn_smfmac_f32_32x32x32_bf16(v, __builtin_bit_cast(bf16x16, h), o, idx, 0, 0);
}

template <class Mid>
__device__ __forceinline__ void qkv_tiles(const lds_char* xb, const lds_char* wb, int l31, int hh, f32x16& aq, f32x16& ak, f32x16& av, Mid mid) {
  bf16x8 fa[3], fb[3], fc[3], fd[3];
  auto xf = [&](int ks) { return lds_frag(xb + ks * 1024 + ((l31 ^ ((2 * ks + hh) & 7)) * 16)); };
  auto wf = [&](int off) { return tr_frag(wb + off, wb + off + 512); };
  auto rd0 = [&](int u) {
    const int o = ((u >> 2) % 6) * QSTG;
    if (u < 32) { fa[u % 3] = xf(u); fb[u % 3] = wf(o + (u & 3) * 2048); }
    else { fa[u % 3] = xf(2 * (u - 32)); fb[u % 3] = wf(o + (u & 3) * 4096); }
  };
  auto rd1 = [&](int u) {
    const int o = ((u >> 2) % 6) * QSTG;
    if (u < 32) fc[u % 3] = wf(o + 8192 + (u & 3) * 2048);
    else { fd[u % 3] = xf(2 * (u - 32) + 1); fc[u % 3] = wf(o + (u & 3) * 4096 + 2048); }
  };
  BARRIER();
  rd0(0); rd1(0); rd0(1); rd1(1);
#pragma unroll
  for (int u = 0; u < 48; ++u) {
    const int p = u % 3;
    if ((u & 3) == 2) { SBAR(); BARRIER(); }
    SBAR();
    if (u < 32) ak = MFMA32(fb[p], fa[p], u ? ak : f32x16{}); else aq = MFMA32(fb[p], fa[p], u > 32 ? aq : f32x16{});
    SBAR();
    if (u < 46) rd0(u + 2);
    SBAR();
    if (u < 32) av = MFMA32(fa[p], fc[p], u ? av : f32x16{}); else aq = MFMA32(fc[p], fd[p], aq);
    SBAR();
    if (u < 46) rd1(u + 2);
    SBAR();
    mid(u);
  }
}

__device__ __forceinline__ void write_partial(lds_char* p, const f32x16 (&acc)[2][2], int l31, int hh) {
#pragma unroll
  for (int i = 0; i < 2; ++i)
#pragma unroll
    for (int j = 0; j < 2; ++j)
#pragma unroll
      for (int r4 = 0; r4 < 4; ++r4) {
        const int tok = i * 32 + l31, ch = j * 8 + 2 * r4 + hh;
        *(LAS f32x4*)(p + tok * 256 + ((ch ^ (tok & 7)) * 16)) = f32x4{acc[i][j][4 * r4], acc[i][j][4 * r4 + 1], acc[i][j][4 * r4 + 2], acc[i][j][4 * r4 + 3]};
      }
}
#define F2_CLOB "memory", "scc", "v64", "v65", "v66", "v67", "v68", "v69", "v70", "v71", "v72", "v73", "v74", "v75", "v76", "v77", "v78", "v79", "v80", "v81", "v82", "v83", "v84", "v85", "v86", "v87", "v88", "v89", "v90", "v91", "v92", "v93", "v94", "v95", "v96", "v97", "v98", "v99", "v100", "v101", "v102", "v103", "v104", "v105", "v106", "v107", "v108", "v109", "v110", "v111", "v112", "v113", "v114", "v115", "v116", "v117", "v118", "v119", "v120", "v121", "v122", "v123", "v124", "v125", "v126", "v127", "v128", "v129", "v130", "v131", "v132", "v133", "v134", "v135", "v136", "v137", "v138", "v139", "v140", "v141", "v142", "v143"

extern "C" __global__ __launch_bounds__(768, 3) void k_fused(const uint16_t* __restrict__ x, const uint16_t* __restrict__ wqkv, const uint16_t* __restrict__ wo,
                                                  const uint16_t* __restrict__ wg, const uint16_t* __restrict__ wu, const uint16_t* __restrict__ wd,
                                                  uint16_t* __restrict__ out, char* __restrict__ ws) {
  extern __shared__ __attribute__((aligned(16))) char smem[];
  lds_char* lds = (lds_char*)smem;
  const unsigned lds0 = (unsigned)(uintptr_t)lds;
#define ARGS() asm volatile("" :: "s"(x), "s"(wqkv), "s"(wo), "s"(wg), "s"(wu), "s"(wd), "s"(out), "s"(ws))
  const int w = __builtin_amdgcn_readfirstlane(threadIdx.x >> 6);
  const int xcd = blockIdx.x & 7, idx = blockIdx.x >> 3;
  const int tb = (xcd >> 1) * 8 + (idx >> 2), cg = (xcd & 1) * 4 + (idx & 3);
  const int cq = (cg & 4) | ((cg + 1) & 3);
#define LANE_VARS() const int lane = fresh_lane(), r8 = lane >> 3, c8 = lane & 7, l31 = lane & 31, hh = lane >> 5, blk = (lane >> 4) & 1, \
                              q4 = (lane & 15) >> 2, p4 = lane & 3; const unsigned l16 = lane * 16
  u64 tag;

  if (w < 8) {
    LANE_VARS();
    ARGS();
    const uint16_t* xrow = x + (size_t)(tb * 64 + w * 8 + r8) * DM + c8 * 8;
    u32x4 xv[8];
#pragma unroll
    for (int i = 0; i < 8; ++i) asm volatile("global_load_dwordx4 %0, %1, off offset:%c2" : "=v"(xv[i]) : "v"(xrow), "i"(i * 128) : "memory");
    asm volatile("s_load_dwordx2 %0, %1, 0x0" : EPOCH_OUT(tag) : "s"(ws + WS_EPOCH) : "memory");
    asm volatile("s_waitcnt vmcnt(0)" : "+v"(xv[0]), "+v"(xv[1]), "+v"(xv[2]), "+v"(xv[3]), "+v"(xv[4]), "+v"(xv[5]), "+v"(xv[6]), "+v"(xv[7]) :: "memory");
    {
      const bf16x2 one2 = {(__bf16)1.0f, (__bf16)1.0f};
      float s0 = 0.f, s1 = 0.f;
#pragma unroll
      for (int i = 0; i < 8; ++i) {
        s0 = __builtin_amdgcn_fdot2_f32_bf16(as_bf2(xv[i][0]), one2, s0, false);
        s1 = __builtin_amdgcn_fdot2_f32_bf16(as_bf2(xv[i][1]), one2, s1, false);
        s0 = __builtin_amdgcn_fdot2_f32_bf16(as_bf2(xv[i][2]), one2, s0, false);
        s1 = __builtin_amdgcn_fdot2_f32_bf16(as_bf2(xv[i][3]), one2, s1, false);
      }
      const float mean = sum8(s0 + s1) * (1.0f / DM), mb = bf_round(mean);
      f32x2 d[32], q0 = {0.f, 0.f}, q1 = {0.f, 0.f};
#pragma unroll
      for (int i = 0; i < 32; ++i) {
        const unsigned u = xv[i >> 2][i & 3];
        d[i] = f32x2{bf_lo(u), bf_hi(u)} - mb;
        if (i & 1) q1 = __builtin_elementwise_fma(d[i], d[i], q1); else q0 = __builtin_elementwise_fma(d[i], d[i], q0);
      }
      q0 += q1;
      const float dm = mean - mb;
      const float var = fmaxf(sum8(q0[0] + q0[1]) * (1.0f / DM) - dm * dm, 0.f);
      const float rs = __builtin_amdgcn_rcpf(bf_round(__builtin_amdgcn_sqrtf(bf_round(bf_round(var) + 1e-5f))));
      const int t = (w * 8 + r8) & 31;
      lds_char* xw = lds + ((w >> 2) * 32 * 64 + (c8 >> 1) * 64 + (c8 & 1) * 32 + (t ^ c8)) * 16;
#pragma unroll
      for (int i = 0; i < 8; ++i) {
        u32x4 o;
#pragma unroll
        for (int j = 0; j < 4; ++j) { const f32x2 e = d[i * 4 + j] * rs; o[j] = pk_bf16(e[0], e[1]); }
        *(LAS u32x4*)(xw + i * 4096) = o;
      }
    }
    asm volatile("s_waitcnt lgkmcnt(0)" : EPOCH_IO(tag) :: "memory");
    tag += 1;
    {
      if (w < 4) {
        const int ttl = w >> 1, hp = w & 1;
        const lds_char* xb = lds + (ttl * 32 * 64 + hh * 32) * 16;
        const lds_char* wb = lds + L_RING + (8 * hh + q4) * 128 + ((64 * hp + 32 * blk + 8 * p4) ^ ((q4 >> 1) << 6));
        const int tt = tb * 2 + ttl, h0 = cq * 4 + hp * 2;
        const int sl = (l31 & 0x13) | ((l31 & 4) << 1) | ((l31 & 8) >> 1);
        char* kv = ws + WS_KV + ((size_t)h0 * 64 + tt) * 2048;
        const unsigned ko = (hh * 32 + sl) * 16, vo = blk * 131072 + hh * 256 + (lane & 15) * 16;
        auto tile_max = [&](const f32x16& a, float sc, int slot) {
          float n0 = 0.f, n1 = 0.f;
#pragma unroll
          for (int r = 0; r < 8; ++r) { n0 = fmaf(a[r], a[r], n0); n1 = fmaf(a[8 + r], a[8 + r], n1); }
          auto sw = __builtin_amdgcn_permlane32_swap(__float_as_uint(n0), __float_as_uint(n1), false, false);
          unsigned u = __float_as_uint((__uint_as_float(sw[0]) + __uint_as_float(sw[1])) * sc) & 0x7fffffffu;
          u = umax16(u);
          auto s16 = __builtin_amdgcn_permlane16_swap(u, u, false, false);
          u = max(s16[0], s16[1]);
          asm volatile("s_mov_b64 exec, %2\n\tds_max_u32 %0, %1\n\ts_mov_b64 exec, -1" :: "v"(lds0 + L_CTL + (C_KM + slot + hp * 2 + hh) * 4), "v"(u), "s"(0x0000000100000001ull) : "memory");
        };
        f32x16 aq, ak, av;
        qkv_tiles(xb, wb, l31, hh, aq, ak, av, [&](int u) {
          if (u == 32) st16_dev(kv, ko, pack8(ak, 0, 1.f));
          if (u == 33) st16_dev(kv + 131072, ko, pack8(ak, 8, 1.f));
          if (u == 34) st16_dev(kv + 1024, vo, pack8(av, 0, 1.f));
          if (u == 35) st16_dev(kv + 1536, vo, pack8(av, 8, 1.f));
          if (u == 39) tile_max(ak, 1.f, 0);
        });
        const float qs = 0.25f * 1.4426950408889634f;
        char* qf = ws + WS_Q + ((size_t)h0 * 64 + tt) * 1024;
        st16_dev(qf, l16, pack8(aq, 0, qs));
        st16_dev(qf + 65536, l16, pack8(aq, 8, qs));
        tile_max(aq, qs * qs, 4);
        asm volatile("s_waitcnt vmcnt(0) lgkmcnt(0)" ::: "memory");
        if (lds_ticket(lds, C_PUB) == 3 && lane < 16) {
          const unsigned k2 = __hip_atomic_load((LAS unsigned*)(lds + L_CTL) + C_KM + (lane & 7), __ATOMIC_RELAXED, __HIP_MEMORY_SCOPE_WORKGROUP);
          const unsigned kb = (k2 + 0xffffu) >> 16;
          __hip_atomic_store((GAS u64*)(ws + WS_FQKV + (cq * 32 + tb) * FS) + lane, (tag << 16) | kb, __ATOMIC_RELAXED, __HIP_MEMORY_SCOPE_AGENT);
        }
      } else {
        BARRIER();
#pragma unroll
        for (int e = 0; e < 2; ++e) {
          const int ttl = (w >> 1) & 1, ks = 4 * cq + 2 * (w & 1) + e;
          const u32x4 v = *(const LAS u32x4*)(lds + ((ttl * 32 + ks) * 64 + hh * 32 + (l31 ^ ((2 * ks + hh) & 7))) * 16);
          st16_dev(ws + WS_XN + ((size_t)(tb * 2 + ttl) * 32 + ks) * 1024, l16, v);
        }
#pragma unroll
        for (int c = 0; c < 12; ++c) {
          if (c == 2) WAIT_VM(0);
          BARRIER();
          if (c == 2 && w == 4 && lane < 16)
            __hip_atomic_store((GAS u64*)(ws + WS_FXN + ((tb >> 1) * 16 + (tb & 1) * 8 + cq) * FS) + lane, tag, __ATOMIC_RELAXED, __HIP_MEMORY_SCOPE_AGENT);
        }
      }
    }
    const int head = xcd * 4 + (idx & 3), qb = idx >> 2;
    const int g = w >> 2, kq = w & 3;
    if (w == 4) {
      const GAS u64* fp = (const GAS u64*)(ws + WS_FQKV + ((head >> 2) * 32 + (lane < 32 ? lane : qb * 4 + (lane & 3))) * FS) + (head & 3) + (lane < 32 ? 0 : 4);
      u64 v = 0;
#pragma nounroll
      for (unsigned s = 0; s < SPIN_G; ++s) {
        v = __hip_atomic_load(fp, __ATOMIC_RELAXED, __HIP_MEMORY_SCOPE_AGENT);
        if (__all((v >> 16) == (tag & 0xffffffffffffull))) break;
        __builtin_amdgcn_s_sleep(1);
      }
      unsigned kb = (unsigned)v & 0xffffu;
      kb = max(kb, dpp_u<0xB1>(kb)); kb = max(kb, dpp_u<0x4E>(kb)); kb = max(kb, dpp_u<0x141>(kb));
      if (lane <= 32 && (lane & 7) == 0) __hip_atomic_store((LAS unsigned*)(lds + L_CTL) + C_KQ + (lane >> 3), kb << 16, __ATOMIC_RELAXED, __HIP_MEMORY_SCOPE_WORKGROUP);
      asm volatile("s_waitcnt lgkmcnt(0)" ::: "memory");
      if (lane == 0) __hip_atomic_store((LAS unsigned*)(lds + L_CTL) + C_KVOK, 1u, __ATOMIC_RELAXED, __HIP_MEMORY_SCOPE_WORKGROUP);
    } else {
      lds_wait(lds, C_KVOK, 1);
    }
    __builtin_amdgcn_s_setprio(3);
    const char* kvb = ws + WS_KV + ((size_t)head * 64 + kq * 16) * 2048;
    const unsigned voff = hh * 256 + (lane & 15) * 16;
    const u64 vmask = 0x0000ffff0000ffffull;
    const unsigned ring = lds0 + kq * A_REG;
    const char* qp = ws + WS_Q + ((size_t)head * 64 + qb * 8 + g * 4) * 1024;
    const float bound = __uint_as_float(__builtin_amdgcn_readfirstlane(__float_as_uint(
        __builtin_amdgcn_sqrtf(*(const volatile LAS float*)(lds + L_CTL + (C_KQ + kq) * 4) * *(const volatile LAS float*)(lds + L_CTL + (C_KQ + 4) * 4)) * 1.02f)));
    const unsigned o1 = 0x3f803f80u & (0u - ((0x00110000u >> l31) & 1u));
    const int sidx = 0xdddd;
    f32x16 o[4];
    float m[4];
    if (__float_as_uint(bound) <= 0x42200000u) {
      const char* kp = kvb + g * 2048 - 4096;
      unsigned nit = 2, nd = 5 + g, th = 1, bud = SPIN_L, keep;
      asm volatile(
      "s_mov_b32 %[keep], m0\n\t"
      "v_add_u32 v80, %[q0], %[ko]\n\t"
      "v_add_u32 v81, %[q1], %[ko]\n\t"
      "v_add_u32 v82, %[q2], %[ko]\n\t"
      "v_add_u32 v83, %[q3], %[ko]\n\t"
      "global_load_dwordx4 v[112:115], v80, %[qp] sc1\n\t"
      "global_load_dwordx4 v[116:119], v81, %[qp] sc1\n\t"
      "global_load_dwordx4 v[120:123], v82, %[qp] sc1\n\t"
      "global_load_dwordx4 v[124:127], v83, %[qp] sc1\n\t"
      "s_lshl_b32 m0, %[g], 11\n\t"
      "s_add_i32 m0, m0, %[ring]\n\t"
      "s_add_u32 s40, s40, 0x1000\n\t"
      "s_addc_u32 s41, s41, 0\n\t"
      "global_load_lds_dwordx4 %[ko], s[40:41] sc1\n\t"
      "global_load_lds_dwordx4 %[ko], s[40:41] offset:1024 sc1\n\t"
      "v_mov_b64 v[0:1], 0\n\t"
      "v_mov_b64 v[2:3], 0\n\t"
      "v_mov_b64 v[4:5], 0\n\t"
      "v_mov_b64 v[6:7], 0\n\t"
      "v_mov_b64 v[8:9], 0\n\t"
      "v_mov_b64 v[10:11], 0\n\t"
      "v_mov_b64 v[12:13], 0\n\t"
      "v_mov_b64 v[14:15], 0\n\t"
      "v_mov_b64 v[16:17], 0\n\t"
      "v_mov_b64 v[18:19], 0\n\t"
      "v_mov_b64 v[20:21], 0\n\t"
      "v_mov_b64 v[22:23], 0\n\t"
      "v_mov_b64 v[24:25], 0\n\t"
      "v_mov_b64 v[26:27], 0\n\t"
      "v_mov_b64 v[28:29], 0\n\t"
      "v_mov_b64 v[30:31], 0\n\t"
      "v_mov_b64 v[32:33], 0\n\t"
      "v_mov_b64 v[34:35], 0\n\t"
      "v_mov_b64 v[36:37], 0\n\t"
      "v_mov_b64 v[38:39], 0\n\t"
      "v_mov_b64 v[40:41], 0\n\t"
      "v_mov_b64 v[42:43], 0\n\t"
      "v_mov_b64 v[44:45], 0\n\t"
      "v_mov_b64 v[46:47], 0\n\t"
      "v_mov_b64 v[48:49], 0\n\t"
      "v_mov_b64 v[50:51], 0\n\t"
      "v_mov_b64 v[52:53], 0\n\t"
      "v_mov_b64 v[54:55], 0\n\t"
      "v_mov_b64 v[56:57], 0\n\t"
      "v_mov_b64 v[58:59], 0\n\t"
      "v_mov_b64 v[60:61], 0\n\t"
      "v_mov_b64 v[62:63], 0\n\t"
      "s_add_i32 m0, m0, 4096\n\t"
      "s_add_u32 s40, s40, 0x1000\n\t"
      "s_addc_u32 s41, s41, 0\n\t"
      "global_load_lds_dwordx4 %[ko], s[40:41] sc1\n\t"
      "global_load_lds_dwordx4 %[ko], s[40:41] offset:1024 sc1\n\t"
      "v_mov_b32 v64, %[nb]\n\t"
      "v_mov_b32 v65, %[nb]\n\t"
      "v_mov_b32 v66, %[nb]\n\t"
      "v_mov_b32 v67, %[nb]\n\t"
      "v_mov_b32 v68, %[nb]\n\t"
      "v_mov_b32 v69, %[nb]\n\t"
      "v_mov_b32 v70, %[nb]\n\t"
      "v_mov_b32 v71, %[nb]\n\t"
      "v_mov_b32 v72, %[nb]\n\t"
      "v_mov_b32 v73, %[nb]\n\t"
      "v_mov_b32 v74, %[nb]\n\t"
      "v_mov_b32 v75, %[nb]\n\t"
      "v_mov_b32 v76, %[nb]\n\t"
      "v_mov_b32 v77, %[nb]\n\t"
      "v_mov_b32 v78, %[nb]\n\t"
      "v_mov_b32 v79, %[nb]\n\t"
      "v_mov_b32 v136, %[on]\n\t"
      "v_mov_b32 v137, %[on]\n\t"
      "v_mov_b32 v138, %[on]\n\t"
      "v_mov_b32 v139, %[on]\n\t"
      "v_mov_b32 v140, %[on]\n\t"
      "v_mov_b32 v141, %[on]\n\t"
      "v_mov_b32 v142, %[on]\n\t"
      "v_mov_b32 v143, %[on]\n\t"
      "v_mov_b32 v144, %[on]\n\t"
      "v_mov_b32 v145, %[on]\n\t"
      "v_mov_b32 v146, %[on]\n\t"
      "v_mov_b32 v147, %[on]\n\t"
      "v_mov_b32 v148, %[on]\n\t"
      "v_mov_b32 v149, %[on]\n\t"
      "v_mov_b32 v150, %[on]\n\t"
      "v_mov_b32 v151, %[on]\n\t"
      "s_cmp_lg_u32 %[g], 0\n\t"
      "s_cbranch_scc1 40f\n\t"
      "s_add_i32 m0, %[ring], 40960\n\t"
      "s_add_u32 s40, s40, 0x1000\n\t"
      "s_addc_u32 s41, s41, 0\n\t"
      "global_load_lds_dwordx4 %[ko], s[40:41] sc1\n\t"
      "global_load_lds_dwordx4 %[ko], s[40:41] offset:1024 sc1\n\t"
      "s_waitcnt vmcnt(4)\n\t"
      "ds_add_u32 %[ls], %[one]\n\t"
      "s_branch 41f\n\t"
      "40:\n\t"
      "s_waitcnt vmcnt(2)\n\t"
      "ds_add_u32 %[ls], %[one]\n\t"
      "v_mov_b32 v152, 0\n\t"
      "v_cmp_gt_u32 vcc, %[th], v152\n\t"
      "s_cbranch_vccnz 18f\n\t"
      "28:\n\t"
      "s_add_u32 %[th], %[th], 1\n\t"
      "41:\n\t"
      "ds_read_b128 v[128:131], %[lk] offset:0\n\t"
      "s_mov_b64 exec, %[vm]\n\t"
      "ds_read_b128 v[136:139], %[lv] offset:1024\n\t"
      "ds_read_b128 v[140:143], %[lv] offset:1536\n\t"
      "s_mov_b64 exec, -1\n\t"
      "ds_read_b32 v152, %[lp]\n\t"
      "s_waitcnt lgkmcnt(0)\n\t"
      "v_mfma_f32_32x32x16_bf16 v[80:95], v[128:131], v[112:115], v[64:79]\n\t"
      "v_mfma_f32_32x32x16_bf16 v[96:111], v[128:131], v[116:119], v[64:79]\n\t"
      "s_setprio 1\n\t"
      "s_nop 7\n\t"
      "s_nop 3\n\t"
      "1:\n\t"
      "v_exp_f32 v80, v80\n\t"
      "s_setprio 2\n\t"
      "v_exp_f32 v81, v81\n\t"
      "v_exp_f32 v82, v82\n\t"
      "v_exp_f32 v83, v83\n\t"
      "v_exp_f32 v84, v84\n\t"
      "v_exp_f32 v85, v85\n\t"
      "v_exp_f32 v86, v86\n\t"
      "v_exp_f32 v87, v87\n\t"
      "v_exp_f32 v88, v88\n\t"
      "v_exp_f32 v89, v89\n\t"
      "v_exp_f32 v90, v90\n\t"
      "v_exp_f32 v91, v91\n\t"
      "v_exp_f32 v92, v92\n\t"
      "v_exp_f32 v93, v93\n\t"
      "v_exp_f32 v94, v94\n\t"
      "v_exp_f32 v95, v95\n\t"
      "s_setprio 1\n\t"
      "v_smfmac_f32_32x32x32_bf16 v[0:15], v[136:139], v[80:87], %[idx]\n\t"
      "v_smfmac_f32_32x32x32_bf16 v[0:15], v[140:143], v[88:95], %[idx]\n\t"
      "v_mfma_f32_32x32x16_bf16 v[80:95], v[128:131], v[120:123], v[64:79]\n\t"
      "s_cmp_lg_u32 %[g], 0\n\t"
      "s_cbranch_scc1 30f\n\t"
      "s_waitcnt vmcnt(2) lgkmcnt(0)\n\t"
      "ds_add_u32 %[ls], %[one]\n\t"
      "s_setprio 3\n\t"
      "v_cmp_gt_u32 vcc, %[th], v152\n\t"
      "s_cbranch_vccnz 10f\n\t"
      "20:\n\t"
      "s_add_u32 %[th], %[th], 1\n\t"
      "s_setprio 1\n\t"
      "30:\n\t"
      "ds_read_b128 v[132:135], %[lk] offset:2048\n\t"
      "s_mov_b64 exec, %[vm]\n\t"
      "ds_read_b128 v[144:147], %[lv] offset:3072\n\t"
      "ds_read_b128 v[148:151], %[lv] offset:3584\n\t"
      "s_mov_b64 exec, -1\n\t"
      "v_exp_f32 v96, v96\n\t"
      "s_setprio 2\n\t"
      "v_exp_f32 v97, v97\n\t"
      "v_exp_f32 v98, v98\n\t"
      "v_exp_f32 v99, v99\n\t"
      "v_exp_f32 v100, v100\n\t"
      "v_exp_f32 v101, v101\n\t"
      "v_exp_f32 v102, v102\n\t"
      "v_exp_f32 v103, v103\n\t"
      "v_exp_f32 v104, v104\n\t"
      "v_exp_f32 v105, v105\n\t"
      "v_exp_f32 v106, v106\n\t"
      "v_exp_f32 v107, v107\n\t"
      "v_exp_f32 v108, v108\n\t"
      "v_exp_f32 v109, v109\n\t"
      "v_exp_f32 v110, v110\n\t"
      "v_exp_f32 v111, v111\n\t"
      "s_setprio 1\n\t"
      "v_smfmac_f32_32x32x32_bf16 v[16:31], v[136:139], v[96:103], %[idx]\n\t"
      "v_smfmac_f32_32x32x32_bf16 v[16:31], v[140:143], v[104:111], %[idx]\n\t"
      "v_mfma_f32_32x32x16_bf16 v[96:111], v[128:131], v[124:127], v[64:79]\n\t"
      "v_exp_f32 v80, v80\n\t"
      "s_setprio 2\n\t"
      "v_exp_f32 v81, v81\n\t"
      "v_exp_f32 v82, v82\n\t"
      "v_exp_f32 v83, v83\n\t"
      "v_exp_f32 v84, v84\n\t"
      "v_exp_f32 v85, v85\n\t"
      "v_exp_f32 v86, v86\n\t"
      "v_exp_f32 v87, v87\n\t"
      "v_exp_f32 v88, v88\n\t"
      "v_exp_f32 v89, v89\n\t"
      "v_exp_f32 v90, v90\n\t"
      "v_exp_f32 v91, v91\n\t"
      "v_exp_f32 v92, v92\n\t"
      "v_exp_f32 v93, v93\n\t"
      "v_exp_f32 v94, v94\n\t"
      "v_exp_f32 v95, v95\n\t"
      "s_setprio 1\n\t"
      "v_smfmac_f32_32x32x32_bf16 v[32:47], v[136:139], v[80:87], %[idx]\n\t"
      "v_smfmac_f32_32x32x32_bf16 v[32:47], v[140:143], v[88:95], %[idx]\n\t"
      "s_waitcnt lgkmcnt(0)\n\t"
      "v_mfma_f32_32x32x16_bf16 v[80:95], v[132:135], v[112:115], v[64:79]\n\t"
      "s_cmp_eq_u32 %[g], 0\n\t"
      "s_cbranch_scc1 50f\n\t"
      "s_sub_u32 %[n], %[n], 1\n\t"
      "s_cbranch_scc1 60f\n\t"
      "s_add_i32 m0, %[ring], 43008\n\t"
      "s_add_u32 s40, s40, 0x1000\n\t"
      "s_addc_u32 s41, s41, 0\n\t"
      "global_load_lds_dwordx4 %[ko], s[40:41] sc1\n\t"
      "global_load_lds_dwordx4 %[ko], s[40:41] offset:1024 sc1\n\t"
      "50:\n\t"
      "v_exp_f32 v96, v96\n\t"
      "s_setprio 2\n\t"
      "v_exp_f32 v97, v97\n\t"
      "v_exp_f32 v98, v98\n\t"
      "v_exp_f32 v99, v99\n\t"
      "v_exp_f32 v100, v100\n\t"
      "v_exp_f32 v101, v101\n\t"
      "v_exp_f32 v102, v102\n\t"
      "v_exp_f32 v103, v103\n\t"
      "v_exp_f32 v104, v104\n\t"
      "v_exp_f32 v105, v105\n\t"
      "v_exp_f32 v106, v106\n\t"
      "v_exp_f32 v107, v107\n\t"
      "v_exp_f32 v108, v108\n\t"
      "v_exp_f32 v109, v109\n\t"
      "v_exp_f32 v110, v110\n\t"
      "v_exp_f32 v111, v111\n\t"
      "s_setprio 1\n\t"
      "v_smfmac_f32_32x32x32_bf16 v[48:63], v[136:139], v[96:103], %[idx]\n\t"
      "v_smfmac_f32_32x32x32_bf16 v[48:63], v[140:143], v[104:111], %[idx]\n\t"
      "v_mfma_f32_32x32x16_bf16 v[96:111], v[132:135], v[116:119], v[64:79]\n\t"
      "ds_read_b32 v152, %[lp]\n\t"
      "v_exp_f32 v80, v80\n\t"
      "s_setprio 2\n\t"
      "v_exp_f32 v81, v81\n\t"
      "v_exp_f32 v82, v82\n\t"
      "v_exp_f32 v83, v83\n\t"
      "v_exp_f32 v84, v84\n\t"
      "v_exp_f32 v85, v85\n\t"
      "v_exp_f32 v86, v86\n\t"
      "v_exp_f32 v87, v87\n\t"
      "v_exp_f32 v88, v88\n\t"
      "v_exp_f32 v89, v89\n\t"
      "v_exp_f32 v90, v90\n\t"
      "v_exp_f32 v91, v91\n\t"
      "v_exp_f32 v92, v92\n\t"
      "v_exp_f32 v93, v93\n\t"
      "v_exp_f32 v94, v94\n\t"
      "v_exp_f32 v95, v95\n\t"
      "s_setprio 1\n\t"
      "v_smfmac_f32_32x32x32_bf16 v[0:15], v[144:147], v[80:87], %[idx]\n\t"
      "v_smfmac_f32_32x32x32_bf16 v[0:15], v[148:151], v[88:95], %[idx]\n\t"
      "v_mfma_f32_32x32x16_bf16 v[80:95], v[132:135], v[120:123], v[64:79]\n\t"
      "s_cmp_lg_u32 %[g], 1\n\t"
      "s_cbranch_scc1 31f\n\t"
      "s_waitcnt vmcnt(2) lgkmcnt(0)\n\t"
      "ds_add_u32 %[ls], %[one]\n\t"
      "s_setprio 3\n\t"
      "v_cmp_gt_u32 vcc, %[th], v152\n\t"
      "s_cbranch_vccnz 11f\n\t"
      "21:\n\t"
      "s_add_u32 %[th], %[th], 1\n\t"
      "s_setprio 1\n\t"
      "31:\n\t"
      "ds_read_b128 v[128:131], %[lk] offset:4096\n\t"
      "s_mov_b64 exec, %[vm]\n\t"
      "ds_read_b128 v[136:139], %[lv] offset:5120\n\t"
      "ds_read_b128 v[140:143], %[lv] offset:5632\n\t"
      "s_mov_b64 exec, -1\n\t"
      "v_exp_f32 v96, v96\n\t"
      "s_setprio 2\n\t"
      "v_exp_f32 v97, v97\n\t"
      "v_exp_f32 v98, v98\n\t"
      "v_exp_f32 v99, v99\n\t"
      "v_exp_f32 v100, v100\n\t"
      "v_exp_f32 v101, v101\n\t"
      "v_exp_f32 v102, v102\n\t"
      "v_exp_f32 v103, v103\n\t"
      "v_exp_f32 v104, v104\n\t"
      "v_exp_f32 v105, v105\n\t"
      "v_exp_f32 v106, v106\n\t"
      "v_exp_f32 v107, v107\n\t"
      "v_exp_f32 v108, v108\n\t"
      "v_exp_f32 v109, v109\n\t"
      "v_exp_f32 v110, v110\n\t"
      "v_exp_f32 v111, v111\n\t"
      "s_setprio 1\n\t"
      "v_smfmac_f32_32x32x32_bf16 v[16:31], v[144:147], v[96:103], %[idx]\n\t"
      "v_smfmac_f32_32x32x32_bf16 v[16:31], v[148:151], v[104:111], %[idx]\n\t"
      "v_mfma_f32_32x32x16_bf16 v[96:111], v[132:135], v[124:127], v[64:79]\n\t"
      "v_exp_f32 v80, v80\n\t"
      "s_setprio 2\n\t"
      "v_exp_f32 v81, v81\n\t"
      "v_exp_f32 v82, v82\n\t"
      "v_exp_f32 v83, v83\n\t"
      "v_exp_f32 v84, v84\n\t"
      "v_exp_f32 v85, v85\n\t"
      "v_exp_f32 v86, v86\n\t"
      "v_exp_f32 v87, v87\n\t"
      "v_exp_f32 v88, v88\n\t"
      "v_exp_f32 v89, v89\n\t"
      "v_exp_f32 v90, v90\n\t"
      "v_exp_f32 v91, v91\n\t"
      "v_exp_f32 v92, v92\n\t"
      "v_exp_f32 v93, v93\n\t"
      "v_exp_f32 v94, v94\n\t"
      "v_exp_f32 v95, v95\n\t"
      "s_setprio 1\n\t"
      "v_smfmac_f32_32x32x32_bf16 v[32:47], v[144:147], v[80:87], %[idx]\n\t"
      "v_smfmac_f32_32x32x32_bf16 v[32:47], v[148:151], v[88:95], %[idx]\n\t"
      "s_waitcnt lgkmcnt(0)\n\t"
      "v_mfma_f32_32x32x16_bf16 v[80:95], v[128:131], v[112:115], v[64:79]\n\t"
      "s_cmp_eq_u32 %[g], 1\n\t"
      "s_cbranch_scc1 51f\n\t"
      "s_sub_u32 %[n], %[n], 1\n\t"
      "s_cbranch_scc1 61f\n\t"
      "s_add_i32 m0, %[ring], 45056\n\t"
      "s_add_u32 s40, s40, 0x1000\n\t"
      "s_addc_u32 s41, s41, 0\n\t"
      "global_load_lds_dwordx4 %[ko], s[40:41] sc1\n\t"
      "global_load_lds_dwordx4 %[ko], s[40:41] offset:1024 sc1\n\t"
      "51:\n\t"
      "v_exp_f32 v96, v96\n\t"
      "s_setprio 2\n\t"
      "v_exp_f32 v97, v97\n\t"
      "v_exp_f32 v98, v98\n\t"
      "v_exp_f32 v99, v99\n\t"
      "v_exp_f32 v100, v100\n\t"
      "v_exp_f32 v101, v101\n\t"
      "v_exp_f32 v102, v102\n\t"
      "v_exp_f32 v103, v103\n\t"
      "v_exp_f32 v104, v104\n\t"
      "v_exp_f32 v105, v105\n\t"
      "v_exp_f32 v106, v106\n\t"
      "v_exp_f32 v107, v107\n\t"
      "v_exp_f32 v108, v108\n\t"
      "v_exp_f32 v109, v109\n\t"
      "v_exp_f32 v110, v110\n\t"
      "v_exp_f32 v111, v111\n\t"
      "s_setprio 1\n\t"
      "v_smfmac_f32_32x32x32_bf16 v[48:63], v[144:147], v[96:103], %[idx]\n\t"
      "v_smfmac_f32_32x32x32_bf16 v[48:63], v[148:151], v[104:111], %[idx]\n\t"
      "v_mfma_f32_32x32x16_bf16 v[96:111], v[128:131], v[116:119], v[64:79]\n\t"
      "ds_read_b32 v152, %[lp]\n\t"
      "v_exp_f32 v80, v80\n\t"
      "s_setprio 2\n\t"
      "v_exp_f32 v81, v81\n\t"
      "v_exp_f32 v82, v82\n\t"
      "v_exp_f32 v83, v83\n\t"
      "v_exp_f32 v84, v84\n\t"
      "v_exp_f32 v85, v85\n\t"
      "v_exp_f32 v86, v86\n\t"
      "v_exp_f32 v87, v87\n\t"
      "v_exp_f32 v88, v88\n\t"
      "v_exp_f32 v89, v89\n\t"
      "v_exp_f32 v90, v90\n\t"
      "v_exp_f32 v91, v91\n\t"
      "v_exp_f32 v92, v92\n\t"
      "v_exp_f32 v93, v93\n\t"
      "v_exp_f32 v94, v94\n\t"
      "v_exp_f32 v95, v95\n\t"
      "s_setprio 1\n\t"
      "v_smfmac_f32_32x32x32_bf16 v[0:15], v[136:139], v[80:87], %[idx]\n\t"
      "v_smfmac_f32_32x32x32_bf16 v[0:15], v[140:143], v[88:95], %[idx]\n\t"
      "v_mfma_f32_32x32x16_bf16 v[80:95], v[128:131], v[120:123], v[64:79]\n\t"
      "s_cmp_lg_u32 %[g], 0\n\t"
      "s_cbranch_scc1 32f\n\t"
      "s_waitcnt vmcnt(2) lgkmcnt(0)\n\t"
      "ds_add_u32 %[ls], %[one]\n\t"
      "s_setprio 3\n\t"
      "v_cmp_gt_u32 vcc, %[th], v152\n\t"
      "s_cbranch_vccnz 12f\n\t"
      "22:\n\t"
      "s_add_u32 %[th], %[th], 1\n\t"
      "s_setprio 1\n\t"
      "32:\n\t"
      "ds_read_b128 v[132:135], %[lk] offset:6144\n\t"
      "s_mov_b64 exec, %[vm]\n\t"
      "ds_read_b128 v[144:147], %[lv] offset:7168\n\t"
      "ds_read_b128 v[148:151], %[lv] offset:7680\n\t"
      "s_mov_b64 exec, -1\n\t"
      "v_exp_f32 v96, v96\n\t"
      "s_setprio 2\n\t"
      "v_exp_f32 v97, v97\n\t"
      "v_exp_f32 v98, v98\n\t"
      "v_exp_f32 v99, v99\n\t"
      "v_exp_f32 v100, v100\n\t"
      "v_exp_f32 v101, v101\n\t"
      "v_exp_f32 v102, v102\n\t"
      "v_exp_f32 v103, v103\n\t"
      "v_exp_f32 v104, v104\n\t"
      "v_exp_f32 v105, v105\n\t"
      "v_exp_f32 v106, v106\n\t"
      "v_exp_f32 v107, v107\n\t"
      "v_exp_f32 v108, v108\n\t"
      "v_exp_f32 v109, v109\n\t"
      "v_exp_f32 v110, v110\n\t"
      "v_exp_f32 v111, v111\n\t"
      "s_setprio 1\n\t"
      "v_smfmac_f32_32x32x32_bf16 v[16:31], v[136:139], v[96:103], %[idx]\n\t"
      "v_smfmac_f32_32x32x32_bf16 v[16:31], v[140:143], v[104:111], %[idx]\n\t"
      "v_mfma_f32_32x32x16_bf16 v[96:111], v[128:131], v[124:127], v[64:79]\n\t"
      "v_exp_f32 v80, v80\n\t"
      "s_setprio 2\n\t"
      "v_exp_f32 v81, v81\n\t"
      "v_exp_f32 v82, v82\n\t"
      "v_exp_f32 v83, v83\n\t"
      "v_exp_f32 v84, v84\n\t"
      "v_exp_f32 v85, v85\n\t"
      "v_exp_f32 v86, v86\n\t"
      "v_exp_f32 v87, v87\n\t"
      "v_exp_f32 v88, v88\n\t"
      "v_exp_f32 v89, v89\n\t"
      "v_exp_f32 v90, v90\n\t"
      "v_exp_f32 v91, v91\n\t"
      "v_exp_f32 v92, v92\n\t"
      "v_exp_f32 v93, v93\n\t"
      "v_exp_f32 v94, v94\n\t"
      "v_exp_f32 v95, v95\n\t"
      "s_setprio 1\n\t"
      "v_smfmac_f32_32x32x32_bf16 v[32:47], v[136:139], v[80:87], %[idx]\n\t"
      "v_smfmac_f32_32x32x32_bf16 v[32:47], v[140:143], v[88:95], %[idx]\n\t"
      "s_waitcnt lgkmcnt(0)\n\t"
      "v_mfma_f32_32x32x16_bf16 v[80:95], v[132:135], v[112:115], v[64:79]\n\t"
      "s_cmp_eq_u32 %[g], 0\n\t"
      "s_cbranch_scc1 52f\n\t"
      "s_sub_u32 %[n], %[n], 1\n\t"
      "s_cbranch_scc1 62f\n\t"
      "s_add_i32 m0, %[ring], 47104\n\t"
      "s_add_u32 s40, s40, 0x1000\n\t"
      "s_addc_u32 s41, s41, 0\n\t"
      "global_load_lds_dwordx4 %[ko], s[40:41] sc1\n\t"
      "global_load_lds_dwordx4 %[ko], s[40:41] offset:1024 sc1\n\t"
      "52:\n\t"
      "v_exp_f32 v96, v96\n\t"
      "s_setprio 2\n\t"
      "v_exp_f32 v97, v97\n\t"
      "v_exp_f32 v98, v98\n\t"
      "v_exp_f32 v99, v99\n\t"
      "v_exp_f32 v100, v100\n\t"
      "v_exp_f32 v101, v101\n\t"
      "v_exp_f32 v102, v102\n\t"
      "v_exp_f32 v103, v103\n\t"
      "v_exp_f32 v104, v104\n\t"
      "v_exp_f32 v105, v105\n\t"
      "v_exp_f32 v106, v106\n\t"
      "v_exp_f32 v107, v107\n\t"
      "v_exp_f32 v108, v108\n\t"
      "v_exp_f32 v109, v109\n\t"
      "v_exp_f32 v110, v110\n\t"
      "v_exp_f32 v111, v111\n\t"
      "s_setprio 1\n\t"
      "v_smfmac_f32_32x32x32_bf16 v[48:63], v[136:139], v[96:103], %[idx]\n\t"
      "v_smfmac_f32_32x32x32_bf16 v[48:63], v[140:143], v[104:111], %[idx]\n\t"
      "v_mfma_f32_32x32x16_bf16 v[96:111], v[132:135], v[116:119], v[64:79]\n\t"
      "ds_read_b32 v152, %[lp]\n\t"
      "v_exp_f32 v80, v80\n\t"
      "s_setprio 2\n\t"
      "v_exp_f32 v81, v81\n\t"
      "v_exp_f32 v82, v82\n\t"
      "v_exp_f32 v83, v83\n\t"
      "v_exp_f32 v84, v84\n\t"
      "v_exp_f32 v85, v85\n\t"
      "v_exp_f32 v86, v86\n\t"
      "v_exp_f32 v87, v87\n\t"
      "v_exp_f32 v88, v88\n\t"
      "v_exp_f32 v89, v89\n\t"
      "v_exp_f32 v90, v90\n\t"
      "v_exp_f32 v91, v91\n\t"
      "v_exp_f32 v92, v92\n\t"
      "v_exp_f32 v93, v93\n\t"
      "v_exp_f32 v94, v94\n\t"
      "v_exp_f32 v95, v95\n\t"
      "s_setprio 1\n\t"
      "v_smfmac_f32_32x32x32_bf16 v[0:15], v[144:147], v[80:87], %[idx]\n\t"
      "v_smfmac_f32_32x32x32_bf16 v[0:15], v[148:151], v[88:95], %[idx]\n\t"
      "v_mfma_f32_32x32x16_bf16 v[80:95], v[132:135], v[120:123], v[64:79]\n\t"
      "s_cmp_lg_u32 %[g], 1\n\t"
      "s_cbranch_scc1 33f\n\t"
      "s_waitcnt vmcnt(2) lgkmcnt(0)\n\t"
      "ds_add_u32 %[ls], %[one]\n\t"
      "s_setprio 3\n\t"
      "v_cmp_gt_u32 vcc, %[th], v152\n\t"
      "s_cbranch_vccnz 13f\n\t"
      "23:\n\t"
      "s_add_u32 %[th], %[th], 1\n\t"
      "s_setprio 1\n\t"
      "33:\n\t"
      "ds_read_b128 v[128:131], %[lk] offset:40960\n\t"
      "s_mov_b64 exec, %[vm]\n\t"
      "ds_read_b128 v[136:139], %[lv] offset:41984\n\t"
      "ds_read_b128 v[140:143], %[lv] offset:42496\n\t"
      "s_mov_b64 exec, -1\n\t"
      "v_exp_f32 v96, v96\n\t"
      "s_setprio 2\n\t"
      "v_exp_f32 v97, v97\n\t"
      "v_exp_f32 v98, v98\n\t"
      "v_exp_f32 v99, v99\n\t"
      "v_exp_f32 v100, v100\n\t"
      "v_exp_f32 v101, v101\n\t"
      "v_exp_f32 v102, v102\n\t"
      "v_exp_f32 v103, v103\n\t"
      "v_exp_f32 v104, v104\n\t"
      "v_exp_f32 v105, v105\n\t"
      "v_exp_f32 v106, v106\n\t"
      "v_exp_f32 v107, v107\n\t"
      "v_exp_f32 v108, v108\n\t"
      "v_exp_f32 v109, v109\n\t"
      "v_exp_f32 v110, v110\n\t"
      "v_exp_f32 v111, v111\n\t"
      "s_setprio 1\n\t"
      "v_smfmac_f32_32x32x32_bf16 v[16:31], v[144:147], v[96:103], %[idx]\n\t"
      "v_smfmac_f32_32x32x32_bf16 v[16:31], v[148:151], v[104:111], %[idx]\n\t"
      "v_mfma_f32_32x32x16_bf16 v[96:111], v[132:135], v[124:127], v[64:79]\n\t"
      "v_exp_f32 v80, v80\n\t"
      "s_setprio 2\n\t"
      "v_exp_f32 v81, v81\n\t"
      "v_exp_f32 v82, v82\n\t"
      "v_exp_f32 v83, v83\n\t"
      "v_exp_f32 v84, v84\n\t"
      "v_exp_f32 v85, v85\n\t"
      "v_exp_f32 v86, v86\n\t"
      "v_exp_f32 v87, v87\n\t"
      "v_exp_f32 v88, v88\n\t"
      "v_exp_f32 v89, v89\n\t"
      "v_exp_f32 v90, v90\n\t"
      "v_exp_f32 v91, v91\n\t"
      "v_exp_f32 v92, v92\n\t"
      "v_exp_f32 v93, v93\n\t"
      "v_exp_f32 v94, v94\n\t"
      "v_exp_f32 v95, v95\n\t"
      "s_setprio 1\n\t"
      "v_smfmac_f32_32x32x32_bf16 v[32:47], v[144:147], v[80:87], %[idx]\n\t"
      "v_smfmac_f32_32x32x32_bf16 v[32:47], v[148:151], v[88:95], %[idx]\n\t"
      "s_waitcnt lgkmcnt(0)\n\t"
      "v_mfma_f32_32x32x16_bf16 v[80:95], v[128:131], v[112:115], v[64:79]\n\t"
      "s_cmp_eq_u32 %[g], 1\n\t"
      "s_cbranch_scc1 53f\n\t"
      "s_sub_u32 %[n], %[n], 1\n\t"
      "s_cbranch_scc1 63f\n\t"
      "s_add_i32 m0, %[ring], 0\n\t"
      "s_add_u32 s40, s40, 0x1000\n\t"
      "s_addc_u32 s41, s41, 0\n\t"
      "global_load_lds_dwordx4 %[ko], s[40:41] sc1\n\t"
      "global_load_lds_dwordx4 %[ko], s[40:41] offset:1024 sc1\n\t"
      "53:\n\t"
      "v_exp_f32 v96, v96\n\t"
      "s_setprio 2\n\t"
      "v_exp_f32 v97, v97\n\t"
      "v_exp_f32 v98, v98\n\t"
      "v_exp_f32 v99, v99\n\t"
      "v_exp_f32 v100, v100\n\t"
      "v_exp_f32 v101, v101\n\t"
      "v_exp_f32 v102, v102\n\t"
      "v_exp_f32 v103, v103\n\t"
      "v_exp_f32 v104, v104\n\t"
      "v_exp_f32 v105, v105\n\t"
      "v_exp_f32 v106, v106\n\t"
      "v_exp_f32 v107, v107\n\t"
      "v_exp_f32 v108, v108\n\t"
      "v_exp_f32 v109, v109\n\t"
      "v_exp_f32 v110, v110\n\t"
      "v_exp_f32 v111, v111\n\t"
      "s_setprio 1\n\t"
      "v_smfmac_f32_32x32x32_bf16 v[48:63], v[144:147], v[96:103], %[idx]\n\t"
      "v_smfmac_f32_32x32x32_bf16 v[48:63], v[148:151], v[104:111], %[idx]\n\t"
      "v_mfma_f32_32x32x16_bf16 v[96:111], v[128:131], v[116:119], v[64:79]\n\t"
      "ds_read_b32 v152, %[lp]\n\t"
      "v_exp_f32 v80, v80\n\t"
      "s_setprio 2\n\t"
      "v_exp_f32 v81, v81\n\t"
      "v_exp_f32 v82, v82\n\t"
      "v_exp_f32 v83, v83\n\t"
      "v_exp_f32 v84, v84\n\t"
      "v_exp_f32 v85, v85\n\t"
      "v_exp_f32 v86, v86\n\t"
      "v_exp_f32 v87, v87\n\t"
      "v_exp_f32 v88, v88\n\t"
      "v_exp_f32 v89, v89\n\t"
      "v_exp_f32 v90, v90\n\t"
      "v_exp_f32 v91, v91\n\t"
      "v_exp_f32 v92, v92\n\t"
      "v_exp_f32 v93, v93\n\t"
      "v_exp_f32 v94, v94\n\t"
      "v_exp_f32 v95, v95\n\t"
      "s_setprio 1\n\t"
      "v_smfmac_f32_32x32x32_bf16 v[0:15], v[136:139], v[80:87], %[idx]\n\t"
      "v_smfmac_f32_32x32x32_bf16 v[0:15], v[140:143], v[88:95], %[idx]\n\t"
      "v_mfma_f32_32x32x16_bf16 v[80:95], v[128:131], v[120:123], v[64:79]\n\t"
      "s_cmp_lg_u32 %[g], 0\n\t"
      "s_cbranch_scc1 34f\n\t"
      "s_waitcnt vmcnt(2) lgkmcnt(0)\n\t"
      "ds_add_u32 %[ls], %[one]\n\t"
      "s_setprio 3\n\t"
      "v_cmp_gt_u32 vcc, %[th], v152\n\t"
      "s_cbranch_vccnz 14f\n\t"
      "24:\n\t"
      "s_add_u32 %[th], %[th], 1\n\t"
      "s_setprio 1\n\t"
      "34:\n\t"
      "ds_read_b128 v[132:135], %[lk] offset:43008\n\t"
      "s_mov_b64 exec, %[vm]\n\t"
      "ds_read_b128 v[144:147], %[lv] offset:44032\n\t"
      "ds_read_b128 v[148:151], %[lv] offset:44544\n\t"
      "s_mov_b64 exec, -1\n\t"
      "v_exp_f32 v96, v96\n\t"
      "s_setprio 2\n\t"
      "v_exp_f32 v97, v97\n\t"
      "v_exp_f32 v98, v98\n\t"
      "v_exp_f32 v99, v99\n\t"
      "v_exp_f32 v100, v100\n\t"
      "v_exp_f32 v101, v101\n\t"
      "v_exp_f32 v102, v102\n\t"
      "v_exp_f32 v103, v103\n\t"
      "v_exp_f32 v104, v104\n\t"
      "v_exp_f32 v105, v105\n\t"
      "v_exp_f32 v106, v106\n\t"
      "v_exp_f32 v107, v107\n\t"
      "v_exp_f32 v108, v108\n\t"
      "v_exp_f32 v109, v109\n\t"
      "v_exp_f32 v110, v110\n\t"
      "v_exp_f32 v111, v111\n\t"
      "s_setprio 1\n\t"
      "v_smfmac_f32_32x32x32_bf16 v[16:31], v[136:139], v[96:103], %[idx]\n\t"
      "v_smfmac_f32_32x32x32_bf16 v[16:31], v[140:143], v[104:111], %[idx]\n\t"
      "v_mfma_f32_32x32x16_bf16 v[96:111], v[128:131], v[124:127], v[64:79]\n\t"
      "v_exp_f32 v80, v80\n\t"
      "s_setprio 2\n\t"
      "v_exp_f32 v81, v81\n\t"
      "v_exp_f32 v82, v82\n\t"
      "v_exp_f32 v83, v83\n\t"
      "v_exp_f32 v84, v84\n\t"
      "v_exp_f32 v85, v85\n\t"
      "v_exp_f32 v86, v86\n\t"
      "v_exp_f32 v87, v87\n\t"
      "v_exp_f32 v88, v88\n\t"
      "v_exp_f32 v89, v89\n\t"
      "v_exp_f32 v90, v90\n\t"
      "v_exp_f32 v91, v91\n\t"
      "v_exp_f32 v92, v92\n\t"
      "v_exp_f32 v93, v93\n\t"
      "v_exp_f32 v94, v94\n\t"
      "v_exp_f32 v95, v95\n\t"
      "s_setprio 1\n\t"
      "v_smfmac_f32_32x32x32_bf16 v[32:47], v[136:139], v[80:87], %[idx]\n\t"
      "v_smfmac_f32_32x32x32_bf16 v[32:47], v[140:143], v[88:95], %[idx]\n\t"
      "s_waitcnt lgkmcnt(0)\n\t"
      "v_mfma_f32_32x32x16_bf16 v[80:95], v[132:135], v[112:115], v[64:79]\n\t"
      "s_cmp_eq_u32 %[g], 0\n\t"
      "s_cbranch_scc1 54f\n\t"
      "s_sub_u32 %[n], %[n], 1\n\t"
      "s_cbranch_scc1 64f\n\t"
      "s_add_i32 m0, %[ring], 2048\n\t"
      "s_add_u32 s40, s40, 0x1000\n\t"
      "s_addc_u32 s41, s41, 0\n\t"
      "global_load_lds_dwordx4 %[ko], s[40:41] sc1\n\t"
      "global_load_lds_dwordx4 %[ko], s[40:41] offset:1024 sc1\n\t"
      "54:\n\t"
      "v_exp_f32 v96, v96\n\t"
      "s_setprio 2\n\t"
      "v_exp_f32 v97, v97\n\t"
      "v_exp_f32 v98, v98\n\t"
      "v_exp_f32 v99, v99\n\t"
      "v_exp_f32 v100, v100\n\t"
      "v_exp_f32 v101, v101\n\t"
      "v_exp_f32 v102, v102\n\t"
      "v_exp_f32 v103, v103\n\t"
      "v_exp_f32 v104, v104\n\t"
      "v_exp_f32 v105, v105\n\t"
      "v_exp_f32 v106, v106\n\t"
      "v_exp_f32 v107, v107\n\t"
      "v_exp_f32 v108, v108\n\t"
      "v_exp_f32 v109, v109\n\t"
      "v_exp_f32 v110, v110\n\t"
      "v_exp_f32 v111, v111\n\t"
      "s_setprio 1\n\t"
      "v_smfmac_f32_32x32x32_bf16 v[48:63], v[136:139], v[96:103], %[idx]\n\t"
      "v_smfmac_f32_32x32x32_bf16 v[48:63], v[140:143], v[104:111], %[idx]\n\t"
      "v_mfma_f32_32x32x16_bf16 v[96:111], v[132:135], v[116:119], v[64:79]\n\t"
      "ds_read_b32 v152, %[lp]\n\t"
      "v_exp_f32 v80, v80\n\t"
      "s_setprio 2\n\t"
      "v_exp_f32 v81, v81\n\t"
      "v_exp_f32 v82, v82\n\t"
      "v_exp_f32 v83, v83\n\t"
      "v_exp_f32 v84, v84\n\t"
      "v_exp_f32 v85, v85\n\t"
      "v_exp_f32 v86, v86\n\t"
      "v_exp_f32 v87, v87\n\t"
      "v_exp_f32 v88, v88\n\t"
      "v_exp_f32 v89, v89\n\t"
      "v_exp_f32 v90, v90\n\t"
      "v_exp_f32 v91, v91\n\t"
      "v_exp_f32 v92, v92\n\t"
      "v_exp_f32 v93, v93\n\t"
      "v_exp_f32 v94, v94\n\t"
      "v_exp_f32 v95, v95\n\t"
      "s_setprio 1\n\t"
      "v_smfmac_f32_32x32x32_bf16 v[0:15], v[144:147], v[80:87], %[idx]\n\t"
      "v_smfmac_f32_32x32x32_bf16 v[0:15], v[148:151], v[88:95], %[idx]\n\t"
      "v_mfma_f32_32x32x16_bf16 v[80:95], v[132:135], v[120:123], v[64:79]\n\t"
      "s_cmp_lg_u32 %[g], 1\n\t"
      "s_cbranch_scc1 35f\n\t"
      "s_waitcnt vmcnt(2) lgkmcnt(0)\n\t"
      "ds_add_u32 %[ls], %[one]\n\t"
      "s_setprio 3\n\t"
      "v_cmp_gt_u32 vcc, %[th], v152\n\t"
      "s_cbranch_vccnz 15f\n\t"
      "25:\n\t"
      "s_add_u32 %[th], %[th], 1\n\t"
      "s_setprio 1\n\t"
      "35:\n\t"
      "ds_read_b128 v[128:131], %[lk] offset:45056\n\t"
      "s_mov_b64 exec, %[vm]\n\t"
      "ds_read_b128 v[136:139], %[lv] offset:46080\n\t"
      "ds_read_b128 v[140:143], %[lv] offset:46592\n\t"
      "s_mov_b64 exec, -1\n\t"
      "v_exp_f32 v96, v96\n\t"
      "s_setprio 2\n\t"
      "v_exp_f32 v97, v97\n\t"
      "v_exp_f32 v98, v98\n\t"
      "v_exp_f32 v99, v99\n\t"
      "v_exp_f32 v100, v100\n\t"
      "v_exp_f32 v101, v101\n\t"
      "v_exp_f32 v102, v102\n\t"
      "v_exp_f32 v103, v103\n\t"
      "v_exp_f32 v104, v104\n\t"
      "v_exp_f32 v105, v105\n\t"
      "v_exp_f32 v106, v106\n\t"
      "v_exp_f32 v107, v107\n\t"
      "v_exp_f32 v108, v108\n\t"
      "v_exp_f32 v109, v109\n\t"
      "v_exp_f32 v110, v110\n\t"
      "v_exp_f32 v111, v111\n\t"
      "s_setprio 1\n\t"
      "v_smfmac_f32_32x32x32_bf16 v[16:31], v[144:147], v[96:103], %[idx]\n\t"
      "v_smfmac_f32_32x32x32_bf16 v[16:31], v[148:151], v[104:111], %[idx]\n\t"
      "v_mfma_f32_32x32x16_bf16 v[96:111], v[132:135], v[124:127], v[64:79]\n\t"
      "v_exp_f32 v80, v80\n\t"
      "s_setprio 2\n\t"
      "v_exp_f32 v81, v81\n\t"
      "v_exp_f32 v82, v82\n\t"
      "v_exp_f32 v83, v83\n\t"
      "v_exp_f32 v84, v84\n\t"
      "v_exp_f32 v85, v85\n\t"
      "v_exp_f32 v86, v86\n\t"
      "v_exp_f32 v87, v87\n\t"
      "v_exp_f32 v88, v88\n\t"
      "v_exp_f32 v89, v89\n\t"
      "v_exp_f32 v90, v90\n\t"
      "v_exp_f32 v91, v91\n\t"
      "v_exp_f32 v92, v92\n\t"
      "v_exp_f32 v93, v93\n\t"
      "v_exp_f32 v94, v94\n\t"
      "v_exp_f32 v95, v95\n\t"
      "s_setprio 1\n\t"
      "v_smfmac_f32_32x32x32_bf16 v[32:47], v[144:147], v[80:87], %[idx]\n\t"
      "v_smfmac_f32_32x32x32_bf16 v[32:47], v[148:151], v[88:95], %[idx]\n\t"
      "s_waitcnt lgkmcnt(0)\n\t"
      "v_mfma_f32_32x32x16_bf16 v[80:95], v[128:131], v[112:115], v[64:79]\n\t"
      "s_cmp_eq_u32 %[g], 1\n\t"
      "s_cbranch_scc1 55f\n\t"
      "s_sub_u32 %[n], %[n], 1\n\t"
      "s_cbranch_scc1 65f\n\t"
      "s_add_i32 m0, %[ring], 4096\n\t"
      "s_add_u32 s40, s40, 0x1000\n\t"
      "s_addc_u32 s41, s41, 0\n\t"
      "global_load_lds_dwordx4 %[ko], s[40:41] sc1\n\t"
      "global_load_lds_dwordx4 %[ko], s[40:41] offset:1024 sc1\n\t"
      "55:\n\t"
      "v_exp_f32 v96, v96\n\t"
      "s_setprio 2\n\t"
      "v_exp_f32 v97, v97\n\t"
      "v_exp_f32 v98, v98\n\t"
      "v_exp_f32 v99, v99\n\t"
      "v_exp_f32 v100, v100\n\t"
      "v_exp_f32 v101, v101\n\t"
      "v_exp_f32 v102, v102\n\t"
      "v_exp_f32 v103, v103\n\t"
      "v_exp_f32 v104, v104\n\t"
      "v_exp_f32 v105, v105\n\t"
      "v_exp_f32 v106, v106\n\t"
      "v_exp_f32 v107, v107\n\t"
      "v_exp_f32 v108, v108\n\t"
      "v_exp_f32 v109, v109\n\t"
      "v_exp_f32 v110, v110\n\t"
      "v_exp_f32 v111, v111\n\t"
      "s_setprio 1\n\t"
      "v_smfmac_f32_32x32x32_bf16 v[48:63], v[144:147], v[96:103], %[idx]\n\t"
      "v_smfmac_f32_32x32x32_bf16 v[48:63], v[148:151], v[104:111], %[idx]\n\t"
      "v_mfma_f32_32x32x16_bf16 v[96:111], v[128:131], v[116:119], v[64:79]\n\t"
      "ds_read_b32 v152, %[lp]\n\t"
      "v_exp_f32 v80, v80\n\t"
      "s_setprio 2\n\t"
      "v_exp_f32 v81, v81\n\t"
      "v_exp_f32 v82, v82\n\t"
      "v_exp_f32 v83, v83\n\t"
      "v_exp_f32 v84, v84\n\t"
      "v_exp_f32 v85, v85\n\t"
      "v_exp_f32 v86, v86\n\t"
      "v_exp_f32 v87, v87\n\t"
      "v_exp_f32 v88, v88\n\t"
      "v_exp_f32 v89, v89\n\t"
      "v_exp_f32 v90, v90\n\t"
      "v_exp_f32 v91, v91\n\t"
      "v_exp_f32 v92, v92\n\t"
      "v_exp_f32 v93, v93\n\t"
      "v_exp_f32 v94, v94\n\t"
      "v_exp_f32 v95, v95\n\t"
      "s_setprio 1\n\t"
      "v_smfmac_f32_32x32x32_bf16 v[0:15], v[136:139], v[80:87], %[idx]\n\t"
      "v_smfmac_f32_32x32x32_bf16 v[0:15], v[140:143], v[88:95], %[idx]\n\t"
      "v_mfma_f32_32x32x16_bf16 v[80:95], v[128:131], v[120:123], v[64:79]\n\t"
      "s_cmp_lg_u32 %[g], 0\n\t"
      "s_cbranch_scc1 36f\n\t"
      "s_waitcnt vmcnt(2) lgkmcnt(0)\n\t"
      "ds_add_u32 %[ls], %[one]\n\t"
      "s_setprio 3\n\t"
      "v_cmp_gt_u32 vcc, %[th], v152\n\t"
      "s_cbranch_vccnz 16f\n\t"
      "26:\n\t"
      "s_add_u32 %[th], %[th], 1\n\t"
      "s_setprio 1\n\t"
      "36:\n\t"
      "ds_read_b128 v[132:135], %[lk] offset:47104\n\t"
      "s_mov_b64 exec, %[vm]\n\t"
      "ds_read_b128 v[144:147], %[lv] offset:48128\n\t"
      "ds_read_b128 v[148:151], %[lv] offset:48640\n\t"
      "s_mov_b64 exec, -1\n\t"
      "v_exp_f32 v96, v96\n\t"
      "s_setprio 2\n\t"
      "v_exp_f32 v97, v97\n\t"
      "v_exp_f32 v98, v98\n\t"
      "v_exp_f32 v99, v99\n\t"
      "v_exp_f32 v100, v100\n\t"
      "v_exp_f32 v101, v101\n\t"
      "v_exp_f32 v102, v102\n\t"
      "v_exp_f32 v103, v103\n\t"
      "v_exp_f32 v104, v104\n\t"
      "v_exp_f32 v105, v105\n\t"
      "v_exp_f32 v106, v106\n\t"
      "v_exp_f32 v107, v107\n\t"
      "v_exp_f32 v108, v108\n\t"
      "v_exp_f32 v109, v109\n\t"
      "v_exp_f32 v110, v110\n\t"
      "v_exp_f32 v111, v111\n\t"
      "s_setprio 1\n\t"
      "v_smfmac_f32_32x32x32_bf16 v[16:31], v[136:139], v[96:103], %[idx]\n\t"
      "v_smfmac_f32_32x32x32_bf16 v[16:31], v[140:143], v[104:111], %[idx]\n\t"
      "v_mfma_f32_32x32x16_bf16 v[96:111], v[128:131], v[124:127], v[64:79]\n\t"
      "v_exp_f32 v80, v80\n\t"
      "s_setprio 2\n\t"
      "v_exp_f32 v81, v81\n\t"
      "v_exp_f32 v82, v82\n\t"
      "v_exp_f32 v83, v83\n\t"
      "v_exp_f32 v84, v84\n\t"
      "v_exp_f32 v85, v85\n\t"
      "v_exp_f32 v86, v86\n\t"
      "v_exp_f32 v87, v87\n\t"
      "v_exp_f32 v88, v88\n\t"
      "v_exp_f32 v89, v89\n\t"
      "v_exp_f32 v90, v90\n\t"
      "v_exp_f32 v91, v91\n\t"
      "v_exp_f32 v92, v92\n\t"
      "v_exp_f32 v93, v93\n\t"
      "v_exp_f32 v94, v94\n\t"
      "v_exp_f32 v95, v95\n\t"
      "s_setprio 1\n\t"
      "v_smfmac_f32_32x32x32_bf16 v[32:47], v[136:139], v[80:87], %[idx]\n\t"
      "v_smfmac_f32_32x32x32_bf16 v[32:47], v[140:143], v[88:95], %[idx]\n\t"
      "s_waitcnt lgkmcnt(0)\n\t"
      "v_mfma_f32_32x32x16_bf16 v[80:95], v[132:135], v[112:115], v[64:79]\n\t"
      "s_cmp_eq_u32 %[g], 0\n\t"
      "s_cbranch_scc1 56f\n\t"
      "s_sub_u32 %[n], %[n], 1\n\t"
      "s_cbranch_scc1 66f\n\t"
      "s_add_i32 m0, %[ring], 6144\n\t"
      "s_add_u32 s40, s40, 0x1000\n\t"
      "s_addc_u32 s41, s41, 0\n\t"
      "global_load_lds_dwordx4 %[ko], s[40:41] sc1\n\t"
      "global_load_lds_dwordx4 %[ko], s[40:41] offset:1024 sc1\n\t"
      "56:\n\t"
      "v_exp_f32 v96, v96\n\t"
      "s_setprio 2\n\t"
      "v_exp_f32 v97, v97\n\t"
      "v_exp_f32 v98, v98\n\t"
      "v_exp_f32 v99, v99\n\t"
      "v_exp_f32 v100, v100\n\t"
      "v_exp_f32 v101, v101\n\t"
      "v_exp_f32 v102, v102\n\t"
      "v_exp_f32 v103, v103\n\t"
      "v_exp_f32 v104, v104\n\t"
      "v_exp_f32 v105, v105\n\t"
      "v_exp_f32 v106, v106\n\t"
      "v_exp_f32 v107, v107\n\t"
      "v_exp_f32 v108, v108\n\t"
      "v_exp_f32 v109, v109\n\t"
      "v_exp_f32 v110, v110\n\t"
      "v_exp_f32 v111, v111\n\t"
      "s_setprio 1\n\t"
      "v_smfmac_f32_32x32x32_bf16 v[48:63], v[136:139], v[96:103], %[idx]\n\t"
      "v_smfmac_f32_32x32x32_bf16 v[48:63], v[140:143], v[104:111], %[idx]\n\t"
      "v_mfma_f32_32x32x16_bf16 v[96:111], v[132:135], v[116:119], v[64:79]\n\t"
      "ds_read_b32 v152, %[lp]\n\t"
      "v_exp_f32 v80, v80\n\t"
      "s_setprio 2\n\t"
      "v_exp_f32 v81, v81\n\t"
      "v_exp_f32 v82, v82\n\t"
      "v_exp_f32 v83, v83\n\t"
      "v_exp_f32 v84, v84\n\t"
      "v_exp_f32 v85, v85\n\t"
      "v_exp_f32 v86, v86\n\t"
      "v_exp_f32 v87, v87\n\t"
      "v_exp_f32 v88, v88\n\t"
      "v_exp_f32 v89, v89\n\t"
      "v_exp_f32 v90, v90\n\t"
      "v_exp_f32 v91, v91\n\t"
      "v_exp_f32 v92, v92\n\t"
      "v_exp_f32 v93, v93\n\t"
      "v_exp_f32 v94, v94\n\t"
      "v_exp_f32 v95, v95\n\t"
      "s_setprio 1\n\t"
      "v_smfmac_f32_32x32x32_bf16 v[0:15], v[144:147], v[80:87], %[idx]\n\t"
      "v_smfmac_f32_32x32x32_bf16 v[0:15], v[148:151], v[88:95], %[idx]\n\t"
      "v_mfma_f32_32x32x16_bf16 v[80:95], v[132:135], v[120:123], v[64:79]\n\t"
      "s_cmp_lg_u32 %[g], 1\n\t"
      "s_cbranch_scc1 37f\n\t"
      "s_waitcnt vmcnt(2) lgkmcnt(0)\n\t"
      "ds_add_u32 %[ls], %[one]\n\t"
      "s_setprio 3\n\t"
      "v_cmp_gt_u32 vcc, %[th], v152\n\t"
      "s_cbranch_vccnz 17f\n\t"
      "27:\n\t"
      "s_add_u32 %[th], %[th], 1\n\t"
      "s_setprio 1\n\t"
      "37:\n\t"
      "ds_read_b128 v[128:131], %[lk] offset:0\n\t"
      "s_mov_b64 exec, %[vm]\n\t"
      "ds_read_b128 v[136:139], %[lv] offset:1024\n\t"
      "ds_read_b128 v[140:143], %[lv] offset:1536\n\t"
      "s_mov_b64 exec, -1\n\t"
      "v_exp_f32 v96, v96\n\t"
      "s_setprio 2\n\t"
      "v_exp_f32 v97, v97\n\t"
      "v_exp_f32 v98, v98\n\t"
      "v_exp_f32 v99, v99\n\t"
      "v_exp_f32 v100, v100\n\t"
      "v_exp_f32 v101, v101\n\t"
      "v_exp_f32 v102, v102\n\t"
      "v_exp_f32 v103, v103\n\t"
      "v_exp_f32 v104, v104\n\t"
      "v_exp_f32 v105, v105\n\t"
      "v_exp_f32 v106, v106\n\t"
      "v_exp_f32 v107, v107\n\t"
      "v_exp_f32 v108, v108\n\t"
      "v_exp_f32 v109, v109\n\t"
      "v_exp_f32 v110, v110\n\t"
      "v_exp_f32 v111, v111\n\t"
      "s_setprio 1\n\t"
      "v_smfmac_f32_32x32x32_bf16 v[16:31], v[144:147], v[96:103], %[idx]\n\t"
      "v_smfmac_f32_32x32x32_bf16 v[16:31], v[148:151], v[104:111], %[idx]\n\t"
      "v_mfma_f32_32x32x16_bf16 v[96:111], v[132:135], v[124:127], v[64:79]\n\t"
      "v_exp_f32 v80, v80\n\t"
      "s_setprio 2\n\t"
      "v_exp_f32 v81, v81\n\t"
      "v_exp_f32 v82, v82\n\t"
      "v_exp_f32 v83, v83\n\t"
      "v_exp_f32 v84, v84\n\t"
      "v_exp_f32 v85, v85\n\t"
      "v_exp_f32 v86, v86\n\t"
      "v_exp_f32 v87, v87\n\t"
      "v_exp_f32 v88, v88\n\t"
      "v_exp_f32 v89, v89\n\t"
      "v_exp_f32 v90, v90\n\t"
      "v_exp_f32 v91, v91\n\t"
      "v_exp_f32 v92, v92\n\t"
      "v_exp_f32 v93, v93\n\t"
      "v_exp_f32 v94, v94\n\t"
      "v_exp_f32 v95, v95\n\t"
      "s_setprio 1\n\t"
      "v_smfmac_f32_32x32x32_bf16 v[32:47], v[144:147], v[80:87], %[idx]\n\t"
      "v_smfmac_f32_32x32x32_bf16 v[32:47], v[148:151], v[88:95], %[idx]\n\t"
      "s_waitcnt lgkmcnt(0)\n\t"
      "v_mfma_f32_32x32x16_bf16 v[80:95], v[128:131], v[112:115], v[64:79]\n\t"
      "s_cmp_eq_u32 %[g], 1\n\t"
      "s_cbranch_scc1 57f\n\t"
      "s_sub_u32 %[n], %[n], 1\n\t"
      "s_cbranch_scc1 67f\n\t"
      "s_add_i32 m0, %[ring], 40960\n\t"
      "s_add_u32 s40, s40, 0x1000\n\t"
      "s_addc_u32 s41, s41, 0\n\t"
      "global_load_lds_dwordx4 %[ko], s[40:41] sc1\n\t"
      "global_load_lds_dwordx4 %[ko], s[40:41] offset:1024 sc1\n\t"
      "57:\n\t"
      "v_exp_f32 v96, v96\n\t"
      "s_setprio 2\n\t"
      "v_exp_f32 v97, v97\n\t"
      "v_exp_f32 v98, v98\n\t"
      "v_exp_f32 v99, v99\n\t"
      "v_exp_f32 v100, v100\n\t"
      "v_exp_f32 v101, v101\n\t"
      "v_exp_f32 v102, v102\n\t"
      "v_exp_f32 v103, v103\n\t"
      "v_exp_f32 v104, v104\n\t"
      "v_exp_f32 v105, v105\n\t"
      "v_exp_f32 v106, v106\n\t"
      "v_exp_f32 v107, v107\n\t"
      "v_exp_f32 v108, v108\n\t"
      "v_exp_f32 v109, v109\n\t"
      "v_exp_f32 v110, v110\n\t"
      "v_exp_f32 v111, v111\n\t"
      "s_setprio 1\n\t"
      "v_smfmac_f32_32x32x32_bf16 v[48:63], v[144:147], v[96:103], %[idx]\n\t"
      "v_smfmac_f32_32x32x32_bf16 v[48:63], v[148:151], v[104:111], %[idx]\n\t"
      "v_mfma_f32_32x32x16_bf16 v[96:111], v[128:131], v[116:119], v[64:79]\n\t"
      "ds_read_b32 v152, %[lp]\n\t"
      "s_add_i32 %[it], %[it], -1\n\t"
      "s_cmp_lg_u32 %[it], 0\n\t"
      "s_cbranch_scc1 1b\n\t"
      "s_setprio 3\n\t"
      "s_waitcnt vmcnt(0) lgkmcnt(0)\n\t"
      "ds_add_u32 %[ls], %[one]\n\t"
      "s_mov_b32 %[th], 10\n\t"
      "v_cmp_gt_u32 vcc, %[th], v152\n\t"
      "s_cbranch_vccnz 19f\n\t"
      "29:\n\t"
      "s_add_u32 %[th], %[th], 1\n\t"
      "s_branch 4f\n\t"
      "18:\n\t"
      "ds_read_b32 v152, %[lp]\n\t"
      "s_sub_u32 %[bud], %[bud], 1\n\t"
      "s_cbranch_scc1 28b\n\t"
      "s_waitcnt lgkmcnt(0)\n\t"
      "v_cmp_gt_u32 vcc, %[th], v152\n\t"
      "s_cbranch_vccnz 18b\n\t"
      "s_branch 28b\n\t"
      "10:\n\t"
      "ds_read_b32 v152, %[lp]\n\t"
      "s_sub_u32 %[bud], %[bud], 1\n\t"
      "s_cbranch_scc1 20b\n\t"
      "s_waitcnt lgkmcnt(0)\n\t"
      "v_cmp_gt_u32 vcc, %[th], v152\n\t"
      "s_cbranch_vccnz 10b\n\t"
      "s_branch 20b\n\t"
      "60:\n\t"
      "s_mov_b32 %[n], 0\n\t"
      "s_waitcnt vmcnt(0)\n\t"
      "s_branch 50b\n\t"
      "11:\n\t"
      "ds_read_b32 v152, %[lp]\n\t"
      "s_sub_u32 %[bud], %[bud], 1\n\t"
      "s_cbranch_scc1 21b\n\t"
      "s_waitcnt lgkmcnt(0)\n\t"
      "v_cmp_gt_u32 vcc, %[th], v152\n\t"
      "s_cbranch_vccnz 11b\n\t"
      "s_branch 21b\n\t"
      "61:\n\t"
      "s_mov_b32 %[n], 0\n\t"
      "s_waitcnt vmcnt(0)\n\t"
      "s_branch 51b\n\t"
      "12:\n\t"
      "ds_read_b32 v152, %[lp]\n\t"
      "s_sub_u32 %[bud], %[bud], 1\n\t"
      "s_cbranch_scc1 22b\n\t"
      "s_waitcnt lgkmcnt(0)\n\t"
      "v_cmp_gt_u32 vcc, %[th], v152\n\t"
      "s_cbranch_vccnz 12b\n\t"
      "s_branch 22b\n\t"
      "62:\n\t"
      "s_mov_b32 %[n], 0\n\t"
      "s_waitcnt vmcnt(0)\n\t"
      "s_branch 52b\n\t"
      "13:\n\t"
      "ds_read_b32 v152, %[lp]\n\t"
      "s_sub_u32 %[bud], %[bud], 1\n\t"
      "s_cbranch_scc1 23b\n\t"
      "s_waitcnt lgkmcnt(0)\n\t"
      "v_cmp_gt_u32 vcc, %[th], v152\n\t"
      "s_cbranch_vccnz 13b\n\t"
      "s_branch 23b\n\t"
      "63:\n\t"
      "s_mov_b32 %[n], 0\n\t"
      "s_waitcnt vmcnt(0)\n\t"
      "s_branch 53b\n\t"
      "14:\n\t"
      "ds_read_b32 v152, %[lp]\n\t"
      "s_sub_u32 %[bud], %[bud], 1\n\t"
      "s_cbranch_scc1 24b\n\t"
      "s_waitcnt lgkmcnt(0)\n\t"
      "v_cmp_gt_u32 vcc, %[th], v152\n\t"
      "s_cbranch_vccnz 14b\n\t"
      "s_branch 24b\n\t"
      "64:\n\t"
      "s_mov_b32 %[n], 0\n\t"
      "s_waitcnt vmcnt(0)\n\t"
      "s_branch 54b\n\t"
      "15:\n\t"
      "ds_read_b32 v152, %[lp]\n\t"
      "s_sub_u32 %[bud], %[bud], 1\n\t"
      "s_cbranch_scc1 25b\n\t"
      "s_waitcnt lgkmcnt(0)\n\t"
      "v_cmp_gt_u32 vcc, %[th], v152\n\t"
      "s_cbranch_vccnz 15b\n\t"
      "s_branch 25b\n\t"
      "65:\n\t"
      "s_mov_b32 %[n], 0\n\t"
      "s_waitcnt vmcnt(0)\n\t"
      "s_branch 55b\n\t"
      "16:\n\t"
      "ds_read_b32 v152, %[lp]\n\t"
      "s_sub_u32 %[bud], %[bud], 1\n\t"
      "s_cbranch_scc1 26b\n\t"
      "s_waitcnt lgkmcnt(0)\n\t"
      "v_cmp_gt_u32 vcc, %[th], v152\n\t"
      "s_cbranch_vccnz 16b\n\t"
      "s_branch 26b\n\t"
      "66:\n\t"
      "s_mov_b32 %[n], 0\n\t"
      "s_waitcnt vmcnt(0)\n\t"
      "s_branch 56b\n\t"
      "17:\n\t"
      "ds_read_b32 v152, %[lp]\n\t"
      "s_sub_u32 %[bud], %[bud], 1\n\t"
      "s_cbranch_scc1 27b\n\t"
      "s_waitcnt lgkmcnt(0)\n\t"
      "v_cmp_gt_u32 vcc, %[th], v152\n\t"
      "s_cbranch_vccnz 17b\n\t"
      "s_branch 27b\n\t"
      "67:\n\t"
      "s_mov_b32 %[n], 0\n\t"
      "s_waitcnt vmcnt(0)\n\t"
      "s_branch 57b\n\t"
      "19:\n\t"
      "ds_read_b32 v152, %[lp]\n\t"
      "s_sub_u32 %[bud], %[bud], 1\n\t"
      "s_cbranch_scc1 29b\n\t"
      "s_waitcnt lgkmcnt(0)\n\t"
      "v_cmp_gt_u32 vcc, %[th], v152\n\t"
      "s_cbranch_vccnz 19b\n\t"
      "s_branch 29b\n\t"
      "4:\n\t"
      "s_mov_b32 m0, %[keep]\n\t"
      "s_nop 7\n\t"
      "s_nop 3\n\t"
      : "=&{v[0:15]}"(o[0]), "=&{v[16:31]}"(o[1]), "=&{v[32:47]}"(o[2]), "=&{v[48:63]}"(o[3]),
        "+{s[40:41]}"(kp), [it] "+s"(nit), [n] "+s"(nd), [th] "+s"(th), [bud] "+s"(bud), [keep] "=&s"(keep)
      : [idx] "v"(sidx), [ko] "v"(l16), [lk] "v"(ring + l16), [lv] "v"(ring + voff), [on] "v"(o1), [nb] "s"(-bound), [vm] "s"(vmask), [ring] "s"(ring), [qp] "s"(qp), [g] "s"(g), [one] "v"(1u),
        [ls] "v"(lds0 + L_KVL + w * 256 + lane * 4), [lp] "v"(lds0 + L_KVL + (w ^ 4) * 256), [q0] "s"(kq * 1024), [q1] "s"(((kq + 1) & 3) * 1024), [q2] "s"(((kq + 2) & 3) * 1024), [q3] "s"(((kq + 3) & 3) * 1024)
      : "memory", "scc", "v64", "v65", "v66", "v67", "v68", "v69", "v70", "v71", "v72", "v73", "v74", "v75", "v76", "v77", "v78", "v79", "v80", "v81", "v82", "v83", "v84", "v85", "v86", "v87", "v88", "v89", "v90", "v91", "v92", "v93", "v94", "v95", "v96", "v97", "v98", "v99", "v100", "v101", "v102", "v103", "v104", "v105", "v106", "v107", "v108", "v109", "v110", "v111", "v112", "v113", "v114", "v115", "v116", "v117", "v118", "v119", "v120", "v121", "v122", "v123", "v124", "v125", "v126", "v127", "v128", "v129", "v130", "v131", "v132", "v133", "v134", "v135", "v136", "v137", "v138", "v139", "v140", "v141", "v142", "v143", "v144", "v145", "v146", "v147", "v148", "v149", "v150", "v151", "v152", "vcc");
#pragma unroll
      for (int i = 0; i < 4; ++i) m[i] = bound;
    } else {
#define LOADK(dst, t) asm volatile("global_load_dwordx4 %0, %1, %2 sc1" : "=v"(dst) : "v"(l16), "s"(kvb + (t) * 2048) : "memory")
#define LOADV(d0, d1, t) asm volatile("s_mov_b64 exec, %4\n\tglobal_load_dwordx4 %0, %2, %3 offset:1024 sc1\n\tglobal_load_dwordx4 %1, %2, %3 offset:1536 sc1\n\ts_mov_b64 exec, -1" \
                                      : "+v"(d0), "+v"(d1) : "v"(voff), "s"(kvb + (t) * 2048), "s"(vmask) : "memory")
      bf16x8 qf[4];
      asm volatile("global_load_dwordx4 %0, %4, %5 sc1\n\tglobal_load_dwordx4 %1, %4, %6 sc1\n\tglobal_load_dwordx4 %2, %4, %7 sc1\n\t"
                   "global_load_dwordx4 %3, %4, %8 sc1\n\ts_waitcnt vmcnt(0)"
                   : "=&v"(qf[0]), "=&v"(qf[1]), "=&v"(qf[2]), "=&v"(qf[3])
                   : "v"(l16), "s"(qp + kq * 1024), "s"(qp + ((kq + 1) & 3) * 1024), "s"(qp + ((kq + 2) & 3) * 1024), "s"(qp + ((kq + 3) & 3) * 1024) : "memory");
      bf16x8 k0, v0 = __builtin_bit_cast(bf16x8, u32x4{o1, o1, o1, o1}), v1 = v0;
#pragma unroll
      for (int i = 0; i < 4; ++i) { m[i] = -INFINITY; o[i] = f32x16{}; }
      for (int t = 0; t < 16; ++t) {
        LOADK(k0, t); LOADV(v0, v1, t);
        asm volatile("s_waitcnt vmcnt(0)" : "+v"(k0), "+v"(v0), "+v"(v1) :: "memory");
#pragma unroll
        for (int i = 0; i < 4; ++i) {
          f32x16 s = MFMA32(k0, qf[i], f32x16{});
          float mx = s[0];
#pragma unroll
          for (int r = 1; r < 16; ++r) mx = fmaxf(mx, s[r]);
          mx = fmaxf(swap_max32(mx), m[i]);
          const float ms = (mx == -INFINITY) ? 0.f : mx;
          const float al = __builtin_amdgcn_exp2f(m[i] - ms);
          m[i] = mx;
#pragma unroll
          for (int r = 0; r < 16; ++r) { s[r] = __builtin_amdgcn_exp2f(s[r] - ms); o[i][r] *= al; }
          o[i] = pv_lo(v0, s, o[i], sidx);
          o[i] = pv_hi(v1, s, o[i], sidx);
        }
      }
    }
    __builtin_amdgcn_s_setprio(3);
    const int lane3 = fresh_lane();
#pragma unroll
    for (int i = 1; i < 4; ++i) {
      lds_char* p = lds + w * A_REG + ((kq + i) & 3) * 2560;
      *(LAS f32x4*)(p + lane3 * 16) = f32x4{o[i][0], o[i][1], o[i][2], o[i][3]};
      *(LAS f32x4*)(p + 1024 + lane3 * 16) = f32x4{o[i][4], o[i][5], o[i][6], o[i][7]};
      *(LAS f32x2*)(p + 2048 + lane3 * 8) = f32x2{o[i][8], m[i]};
    }
    lds_arrive(lds, C_CMB0 + g, lane3);
    {
      f32x4 a[4], b[4]; f32x2 c[4];
      a[0] = f32x4{o[0][0], o[0][1], o[0][2], o[0][3]}; b[0] = f32x4{o[0][4], o[0][5], o[0][6], o[0][7]};
      c[0] = f32x2{o[0][8], m[0]};
      lds_wait_t<0>(lds, C_CMB0 + g, 4);
#pragma unroll
      for (int k = 1; k < 4; ++k) {
        const lds_char* p = lds + (g * 4 + ((kq + k) & 3)) * A_REG + kq * 2560;
        a[k] = *(const LAS f32x4*)(p + lane3 * 16);
        b[k] = *(const LAS f32x4*)(p + 1024 + lane3 * 16);
        c[k] = *(const LAS f32x2*)(p + 2048 + lane3 * 8);
      }
      const float M = fmaxf(fmaxf(c[0][1], c[1][1]), fmaxf(c[2][1], c[3][1]));
      f32x4 sa4 = {}, sb4 = {}; float l = 0.f;
#pragma unroll
      for (int k = 0; k < 4; ++k) { const float wk = __builtin_amdgcn_exp2f(c[k][1] - M); sa4 += a[k] * wk; sb4 += b[k] * wk; l += c[k][0] * wk; }
      const float rl = __builtin_amdgcn_rcpf(l);
      const u32x4 r = {pk_bf16(sa4[0] * rl, sa4[1] * rl), pk_bf16(sa4[2] * rl, sa4[3] * rl), pk_bf16(sb4[0] * rl, sb4[1] * rl), pk_bf16(sb4[2] * rl, sb4[3] * rl)};
      st16_dev(ws + WS_O + ((size_t)(qb * 8 + g * 4 + kq) * 32 + head) * 1024, (lane3 * 16u), r);
    }
    publish(lds, C_OPUB, 8, ws + WS_FO + (qb * 32 + head) * FS, tag, lane3);
    __builtin_amdgcn_s_setprio(0);
    const int tok = w * 8 + (lane3 >> 3);
    const size_t go = (size_t)(tb * 64 + tok) * DM + cg * 64 + (lane3 & 7) * 8;
    const u32x4 xr = *(const u32x4*)(x + go);
    if (w < 4) {
      LANE_VARS();
      const unsigned va = r8 * (DM * 2) + ((c8 ^ (((r8 >> 1) & 1) << 2)) * 16), vb = va + 8 * DM * 2 - 1024;
      const unsigned ring = lds0 + w * 16384;
      const unsigned wb0 = ring + (4 * hh + q4) * 128 + ((32 * blk + 8 * p4) ^ ((q4 >> 1) << 6)), wb1 = wb0 ^ 64;
      const u32x4 ro = make_srd((const char*)wo + cg * 128, DM * DM * 2 - cg * 128);
      lds_wait(lds, C_OPUB, 8);
#pragma unroll
      for (int j = 0; j < 8; ++j) bdma2(ro, (w * 8 + j) * 16384, va, vb, ring + j * 2048);
      wait_flags(ws + WS_FO + ((tb >> 2) * 32 + w * 8 + (lane & 7)) * FS, true, tag);
      const u32x4 rh = make_srd(ws + WS_O + ((size_t)tb * 2 * 32 + w * 8) * 1024, 40 * 1024);
      f32x16 acc[2][2];
      unsigned sh;
      asm volatile(
      "s_movk_i32 %[sh], 0\n\t"
      "buffer_load_dwordx4 v[64:67], %[h0], %[rh], %[sh] offen sc1\n\t"
      "buffer_load_dwordx4 v[68:71], %[h1], %[rh], %[sh] offen sc1\n\t"
      "s_movk_i32 %[sh], 1024\n\t"
      "buffer_load_dwordx4 v[72:75], %[h0], %[rh], %[sh] offen sc1\n\t"
      "buffer_load_dwordx4 v[76:79], %[h1], %[rh], %[sh] offen sc1\n\t"
      "s_movk_i32 %[sh], 2048\n\t"
      "buffer_load_dwordx4 v[80:83], %[h0], %[rh], %[sh] offen sc1\n\t"
      "buffer_load_dwordx4 v[84:87], %[h1], %[rh], %[sh] offen sc1\n\t"
      "s_movk_i32 %[sh], 3072\n\t"
      "buffer_load_dwordx4 v[88:91], %[h0], %[rh], %[sh] offen sc1\n\t"
      "buffer_load_dwordx4 v[92:95], %[h1], %[rh], %[sh] offen sc1\n\t"
      "s_movk_i32 %[sh], 4096\n\t"
      "buffer_load_dwordx4 v[96:99], %[h0], %[rh], %[sh] offen sc1\n\t"
      "buffer_load_dwordx4 v[100:103], %[h1], %[rh], %[sh] offen sc1\n\t"
      "s_movk_i32 %[sh], 5120\n\t"
      "buffer_load_dwordx4 v[104:107], %[h0], %[rh], %[sh] offen sc1\n\t"
      "buffer_load_dwordx4 v[108:111], %[h1], %[rh], %[sh] offen sc1\n\t"
      "s_movk_i32 %[sh], 6144\n\t"
      "buffer_load_dwordx4 v[112:115], %[h0], %[rh], %[sh] offen sc1\n\t"
      "buffer_load_dwordx4 v[116:119], %[h1], %[rh], %[sh] offen sc1\n\t"
      "s_movk_i32 %[sh], 7168\n\t"
      "buffer_load_dwordx4 v[120:123], %[h0], %[rh], %[sh] offen sc1\n\t"
      "buffer_load_dwordx4 v[124:127], %[h1], %[rh], %[sh] offen sc1\n\t"
      "s_waitcnt vmcnt(16)\n\t"
      "ds_read_b64_tr_b16 v[128:129], %[wb0] offset:0\n\t"
      "ds_read_b64_tr_b16 v[130:131], %[wb0] offset:1024\n\t"
      "ds_read_b64_tr_b16 v[132:133], %[wb1] offset:0\n\t"
      "ds_read_b64_tr_b16 v[134:135], %[wb1] offset:1024\n\t"
      "s_waitcnt vmcnt(14) lgkmcnt(0)\n\t"
      "v_mfma_f32_32x32x16_bf16 v[0:15], v[128:131], v[64:67], 0\n\t"
      "ds_read_b64_tr_b16 v[136:137], %[wb0] offset:2048\n\t"
      "ds_read_b64_tr_b16 v[138:139], %[wb0] offset:3072\n\t"
      "v_mfma_f32_32x32x16_bf16 v[16:31], v[132:135], v[64:67], 0\n\t"
      "ds_read_b64_tr_b16 v[140:141], %[wb1] offset:2048\n\t"
      "ds_read_b64_tr_b16 v[142:143], %[wb1] offset:3072\n\t"
      "v_mfma_f32_32x32x16_bf16 v[32:47], v[128:131], v[68:71], 0\n\t"
      "v_mfma_f32_32x32x16_bf16 v[48:63], v[132:135], v[68:71], 0\n\t"
      "s_waitcnt vmcnt(12) lgkmcnt(0)\n\t"
      "v_mfma_f32_32x32x16_bf16 v[0:15], v[136:139], v[72:75], v[0:15]\n\t"
      "ds_read_b64_tr_b16 v[128:129], %[wb0] offset:4096\n\t"
      "ds_read_b64_tr_b16 v[130:131], %[wb0] offset:5120\n\t"
      "v_mfma_f32_32x32x16_bf16 v[16:31], v[140:143], v[72:75], v[16:31]\n\t"
      "ds_read_b64_tr_b16 v[132:133], %[wb1] offset:4096\n\t"
      "ds_read_b64_tr_b16 v[134:135], %[wb1] offset:5120\n\t"
      "v_mfma_f32_32x32x16_bf16 v[32:47], v[136:139], v[76:79], v[32:47]\n\t"
      "v_mfma_f32_32x32x16_bf16 v[48:63], v[140:143], v[76:79], v[48:63]\n\t"
      "s_waitcnt vmcnt(10) lgkmcnt(0)\n\t"
      "v_mfma_f32_32x32x16_bf16 v[0:15], v[128:131], v[80:83], v[0:15]\n\t"
      "ds_read_b64_tr_b16 v[136:137], %[wb0] offset:6144\n\t"
      "ds_read_b64_tr_b16 v[138:139], %[wb0] offset:7168\n\t"
      "v_mfma_f32_32x32x16_bf16 v[16:31], v[132:135], v[80:83], v[16:31]\n\t"
      "ds_read_b64_tr_b16 v[140:141], %[wb1] offset:6144\n\t"
      "ds_read_b64_tr_b16 v[142:143], %[wb1] offset:7168\n\t"
      "v_mfma_f32_32x32x16_bf16 v[32:47], v[128:131], v[84:87], v[32:47]\n\t"
      "v_mfma_f32_32x32x16_bf16 v[48:63], v[132:135], v[84:87], v[48:63]\n\t"
      "s_waitcnt vmcnt(8) lgkmcnt(0)\n\t"
      "v_mfma_f32_32x32x16_bf16 v[0:15], v[136:139], v[88:91], v[0:15]\n\t"
      "ds_read_b64_tr_b16 v[128:129], %[wb0] offset:8192\n\t"
      "ds_read_b64_tr_b16 v[130:131], %[wb0] offset:9216\n\t"
      "v_mfma_f32_32x32x16_bf16 v[16:31], v[140:143], v[88:91], v[16:31]\n\t"
      "ds_read_b64_tr_b16 v[132:133], %[wb1] offset:8192\n\t"
      "ds_read_b64_tr_b16 v[134:135], %[wb1] offset:9216\n\t"
      "v_mfma_f32_32x32x16_bf16 v[32:47], v[136:139], v[92:95], v[32:47]\n\t"
      "v_mfma_f32_32x32x16_bf16 v[48:63], v[140:143], v[92:95], v[48:63]\n\t"
      "s_waitcnt vmcnt(6) lgkmcnt(0)\n\t"
      "v_mfma_f32_32x32x16_bf16 v[0:15], v[128:131], v[96:99], v[0:15]\n\t"
      "ds_read_b64_tr_b16 v[136:137], %[wb0] offset:10240\n\t"
      "ds_read_b64_tr_b16 v[138:139], %[wb0] offset:11264\n\t"
      "v_mfma_f32_32x32x16_bf16 v[16:31], v[132:135], v[96:99], v[16:31]\n\t"
      "ds_read_b64_tr_b16 v[140:141], %[wb1] offset:10240\n\t"
      "ds_read_b64_tr_b16 v[142:143], %[wb1] offset:11264\n\t"
      "v_mfma_f32_32x32x16_bf16 v[32:47], v[128:131], v[100:103], v[32:47]\n\t"
      "v_mfma_f32_32x32x16_bf16 v[48:63], v[132:135], v[100:103], v[48:63]\n\t"
      "s_waitcnt vmcnt(4) lgkmcnt(0)\n\t"
      "v_mfma_f32_32x32x16_bf16 v[0:15], v[136:139], v[104:107], v[0:15]\n\t"
      "ds_read_b64_tr_b16 v[128:129], %[wb0] offset:12288\n\t"
      "ds_read_b64_tr_b16 v[130:131], %[wb0] offset:13312\n\t"
      "v_mfma_f32_32x32x16_bf16 v[16:31], v[140:143], v[104:107], v[16:31]\n\t"
      "ds_read_b64_tr_b16 v[132:133], %[wb1] offset:12288\n\t"
      "ds_read_b64_tr_b16 v[134:135], %[wb1] offset:13312\n\t"
      "v_mfma_f32_32x32x16_bf16 v[32:47], v[136:139], v[108:111], v[32:47]\n\t"
      "v_mfma_f32_32x32x16_bf16 v[48:63], v[140:143], v[108:111], v[48:63]\n\t"
      "s_waitcnt vmcnt(2) lgkmcnt(0)\n\t"
      "v_mfma_f32_32x32x16_bf16 v[0:15], v[128:131], v[112:115], v[0:15]\n\t"
      "ds_read_b64_tr_b16 v[136:137], %[wb0] offset:14336\n\t"
      "ds_read_b64_tr_b16 v[138:139], %[wb0] offset:15360\n\t"
      "v_mfma_f32_32x32x16_bf16 v[16:31], v[132:135], v[112:115], v[16:31]\n\t"
      "ds_read_b64_tr_b16 v[140:141], %[wb1] offset:14336\n\t"
      "ds_read_b64_tr_b16 v[142:143], %[wb1] offset:15360\n\t"
      "v_mfma_f32_32x32x16_bf16 v[32:47], v[128:131], v[116:119], v[32:47]\n\t"
      "v_mfma_f32_32x32x16_bf16 v[48:63], v[132:135], v[116:119], v[48:63]\n\t"
      "s_waitcnt vmcnt(0) lgkmcnt(0)\n\t"
      "v_mfma_f32_32x32x16_bf16 v[0:15], v[136:139], v[120:123], v[0:15]\n\t"
      "v_mfma_f32_32x32x16_bf16 v[16:31], v[140:143], v[120:123], v[16:31]\n\t"
      "v_mfma_f32_32x32x16_bf16 v[32:47], v[136:139], v[124:127], v[32:47]\n\t"
      "v_mfma_f32_32x32x16_bf16 v[48:63], v[140:143], v[124:127], v[48:63]\n\t"
      "s_nop 7\n\t"
      "s_nop 3\n\t"
      : "=&{v[0:15]}"(acc[0][0]), "=&{v[16:31]}"(acc[0][1]), "=&{v[32:47]}"(acc[1][0]), "=&{v[48:63]}"(acc[1][1]), [sh] "=&s"(sh)
      : [wb0] "v"(wb0), [wb1] "v"(wb1), [h0] "v"(l16), [h1] "v"(l16 + 32 * 1024), [rh] "s"(rh)
      : F2_CLOB);
      write_partial(lds + w * 16384, acc, l31, hh);
    }
    BARRIER();
    f32x4 s0 = {}, s1 = {};
#pragma unroll
    for (int k = 0; k < 8; ++k) {
      s0 += *(const LAS f32x4*)(lds + (k < 4 ? k * 16384 : L_F + (k - 4) * 16384) + tok * 256 + (((2 * (lane3 & 7)) ^ (tok & 7)) * 16));
      s1 += *(const LAS f32x4*)(lds + (k < 4 ? k * 16384 : L_F + (k - 4) * 16384) + tok * 256 + (((2 * (lane3 & 7) + 1) ^ (tok & 7)) * 16));
    }
    const u32x4 r = {pk_bf16(s0[0] + bf_lo(xr[0]), s0[1] + bf_hi(xr[0])), pk_bf16(s0[2] + bf_lo(xr[1]), s0[3] + bf_hi(xr[1])),
                     pk_bf16(s1[0] + bf_lo(xr[2]), s1[1] + bf_hi(xr[2])), pk_bf16(s1[2] + bf_lo(xr[3]), s1[3] + bf_hi(xr[3]))};
    *(u32x4*)(out + go) = r;
    if (blockIdx.x == 0 && w == 0 && lane3 == 0) *(u64*)(ws + WS_EPOCH) = tag;
  } else {
    ARGS();
    const int f = w - 8;
    const int T = tb >> 1, fg = (tb & 1) * 8 + cg;
    {
      LANE_VARS();
      const u32x4 rq = make_srd(wqkv, DM * 3 * DM * 2);
      const unsigned qa = r8 * (3 * DM * 2) + ((c8 ^ (((r8 >> 1) & 1) << 2)) * 16), qb2 = qa + 8 * (3 * DM * 2) - 1024;
      auto stage_w = [&](int c) {
        const unsigned dst = lds0 + L_RING + (c % 6) * QSTG;
        if (c < 8) {
          const unsigned so = (c * 64 + f * 16) * (3 * DM * 2) + cq * 128;
          bdma2(rq, so + 1024, qa, qb2, dst + f * 2048);
          bdma2(rq, so + 2048, qa, qb2, dst + 8192 + f * 2048);
        } else {
          const unsigned so = ((c - 8) * 128 + f * 32) * (3 * DM * 2) + cq * 128;
          bdma2(rq, so, qa, qb2, dst + f * 4096);
          bdma2(rq, so + 16 * (3 * DM * 2), qa, qb2, dst + f * 4096 + 2048);
        }
      };
      const u32x4 rg = make_srd((const char*)wg + fg * 224, DM * FF * 2 - fg * 224);
      const u32x4 ru = make_srd((const char*)wu + fg * 224 - 896, DM * FF * 2 - fg * 224 + 896);
      const u32x4 rx = make_srd(ws + WS_XN + (size_t)(T * 4 + f) * 32768 - 1792, 32768 + 1792);
      const unsigned fa = (lane / 14) * (FF * 2) + (lane % 14) * 16;
      const unsigned f1dst = lds0 + L_F + f * F1_CH;
      auto stage_f1 = [&](int s) { bdma_f1w(rg, ru, (s * 16 + f * 4) * (FF * 2), fa, f1dst + s * F1_STG); };
      auto stage_x = [&](int s) { bdma_f1x(rx, s * 1024, l16, f1dst + s * F1_STG); };
      const char* fxn = ws + WS_FXN + (T * 16 + (f >> 1) * 8) * FS;
      u64 xflag;
      stage_w(0); stage_w(1); stage_w(2); stage_w(3); stage_w(4); stage_w(5);
      unsigned tpg;
      unsigned tp2 = 0;
      const unsigned id = blockIdx.x;
      auto touch_ffn = [&]() {
        const void* tb_ = ssel<0>(f, wo, ssel<1>(f, wg, ssel<2>(f, wu, wd)));
        const unsigned n = f == 0 ? 16 : 56;
        asm volatile("global_load_dword %0, %1, %2" : "=v"(tpg) : "v"((id * n + min((unsigned)lane, n - 1)) * 128), "s"(tb_) : "memory");
      };
      {
        const unsigned lo = (id * 2 + (r8 & 1)) * (3 * DM * 2) + c8 * 128, hi = (384 + (id >> 1)) * (3 * DM * 2) + (8 + (id & 1) * 8 + c8) * 128;
        const unsigned msk = 0u - (unsigned)(blk == 0);
        if (f == 0) asm volatile("s_mov_b64 exec, 0xffffff\n\tglobal_load_dword %0, %1, %2\n\ts_mov_b64 exec, -1" : "+v"(tp2) : "v"((lo & msk) | (hi & ~msk)), "s"(wqkv) : "memory");
      }
      WAIT_VM(20);
      BARRIER();
#pragma unroll
      for (int c = 0; c < 12; ++c) {
        if (c <= 6) { WAIT_VM(16); } else if (c == 7) { WAIT_VM(12); } else if (c == 8) { WAIT_VM(8); } else if (c == 9) { WAIT_VM(9); } else if (c == 10) { WAIT_VM(7); }
        BARRIER();
        if (c == 0) touch_ffn();
        if (c < 6) stage_w(c + 6);
        if (c == 3) {
          asm volatile("s_load_dwordx2 %0, %1, 0x0\n\ts_waitcnt lgkmcnt(0)" : "=s"(tag) : "s"(ws + WS_EPOCH) : "memory");
          tag += 1;
        }
        if (c == 7) { if (f == 0) *(LAS unsigned*)(lds + L_CTL + lane * 4) = 0u; *(LAS unsigned*)(lds + L_PRG + f * 256 + lane * 4) = 0u;
          *(LAS unsigned*)(lds + L_KVL + f * 512 + lane * 4) = 0u; *(LAS unsigned*)(lds + L_KVL + f * 512 + 256 + lane * 4) = 0u; }
        if (c == 8) {
          asm volatile("global_load_dwordx2 %0, %1, %2 sc1" : "=v"(xflag) : "v"((lane & 7) * FS), "s"(fxn) : "memory");
          stage_f1(0); stage_f1(1);
        }
        if (c == 9) stage_f1(2);
        if (c == 10) { stage_f1(3); stage_f1(4); }
      }
      asm volatile("s_waitcnt vmcnt(10)" : "+v"(xflag) :: "memory");
      asm volatile("" :: "v"(tpg), "v"(tp2));
      __builtin_amdgcn_s_setprio(3);
      if (!__all(xflag == tag)) wait_flags(fxn + (lane & 7) * FS, true, tag);
#pragma unroll
      for (int s = 0; s < 5; ++s) stage_x(s);
      f32x16 acc[7];
#pragma unroll
      for (int j = 0; j < 7; ++j) { acc[j] = f32x16{}; asm volatile("" : "+v"(acc[j])); }
      WAIT_VM(0);
      const unsigned cw = lds0 + L_PRG + f * 256 + lane * 4, ca = lds0 + L_PRG + (lane & 3) * 256;
      __hip_atomic_store((LAS unsigned*)(uintptr_t)cw, 1u, __ATOMIC_RELAXED, __HIP_MEMORY_SCOPE_WORKGROUP);
      prg_wait(lds, lane, 1);
      {
        const unsigned wa = lds0 + L_F + 2 * hh * F1_CH + blk * 896 + q4 * 224 + 8 * p4;
        unsigned so = (5 * 16 + f * 4) * (FF * 2), sx = (4) * 1024, bud = SPIN_L, thr = 1, keep;
        asm volatile(
      "s_mov_b32 %[keep], m0\n\t"
      "s_setprio 0\n\t"
      "ds_read_b128 v[140:143], %[xa] offset:0\n\t"
      "ds_read_b64_tr_b16 v[112:113], %[wa] offset:0\n\t"
      "ds_read_b64_tr_b16 v[114:115], %[wa] offset:2816\n\t"
      "ds_read_b64_tr_b16 v[116:117], %[wa] offset:32\n\t"
      "ds_read_b64_tr_b16 v[118:119], %[wa] offset:2848\n\t"
      "ds_read_b64_tr_b16 v[120:121], %[wa] offset:64\n\t"
      "ds_read_b64_tr_b16 v[122:123], %[wa] offset:2880\n\t"
      "ds_read_b64_tr_b16 v[124:125], %[wa] offset:96\n\t"
      "ds_read_b64_tr_b16 v[126:127], %[wa] offset:2912\n\t"
      "ds_read_b64_tr_b16 v[128:129], %[wa] offset:128\n\t"
      "ds_read_b64_tr_b16 v[130:131], %[wa] offset:2944\n\t"
      "ds_read_b64_tr_b16 v[132:133], %[wa] offset:160\n\t"
      "ds_read_b64_tr_b16 v[134:135], %[wa] offset:2976\n\t"
      "ds_read_b64_tr_b16 v[136:137], %[wa] offset:192\n\t"
      "ds_read_b64_tr_b16 v[138:139], %[wa] offset:3008\n\t"
      "ds_read_b32 v148, %[ca]\n\t"
      "4:\n\t"
      "s_waitcnt lgkmcnt(9)\n\t"
      "v_mfma_f32_32x32x16_bf16 v[0:15], v[112:115], v[140:143], v[0:15]\n\t"
      "v_mfma_f32_32x32x16_bf16 v[16:31], v[116:119], v[140:143], v[16:31]\n\t"
      "v_mfma_f32_32x32x16_bf16 v[32:47], v[120:123], v[140:143], v[32:47]\n\t"
      "s_waitcnt vmcnt(6) lgkmcnt(0)\n\t"
      "ds_add_u32 %[cw], %[one]\n\t"
      "s_setprio 3\n\t"
      "v_cmp_gt_u32 vcc, %[thr], v148\n\t"
      "s_setprio 0\n\t"
      "s_cbranch_vccnz 10f\n\t"
      "20:\n\t"
      "s_add_i32 m0, %[dst], 56320\n\t"
      "ds_read_b128 v[144:147], %[xa] offset:11264\n\t"
      "ds_read_b64_tr_b16 v[112:113], %[wa] offset:11264\n\t"
      "ds_read_b64_tr_b16 v[114:115], %[wa] offset:14080\n\t"
      "s_add_u32 %[sx], %[sx], 1024\n\t"
      "buffer_load_dwordx4 %[vx], %[rx], %[sx] offen offset:1792 sc1 lds\n\t"
      "ds_read_b64_tr_b16 v[116:117], %[wa] offset:11296\n\t"
      "ds_read_b64_tr_b16 v[118:119], %[wa] offset:14112\n\t"
      "v_mfma_f32_32x32x16_bf16 v[48:63], v[124:127], v[140:143], v[48:63]\n\t"
      "v_mfma_f32_32x32x16_bf16 v[64:79], v[128:131], v[140:143], v[64:79]\n\t"
      "ds_read_b64_tr_b16 v[120:121], %[wa] offset:11328\n\t"
      "ds_read_b64_tr_b16 v[122:123], %[wa] offset:14144\n\t"
      "ds_read_b64_tr_b16 v[124:125], %[wa] offset:11360\n\t"
      "ds_read_b64_tr_b16 v[126:127], %[wa] offset:14176\n\t"
      "ds_read_b64_tr_b16 v[128:129], %[wa] offset:11392\n\t"
      "ds_read_b64_tr_b16 v[130:131], %[wa] offset:14208\n\t"
      "s_mov_b64 exec, %[m56]\n\t"
      "buffer_load_dwordx4 %[va], %[rg], %[so] offen lds\n\t"
      "s_mov_b64 exec, -1\n\t"
      "v_mfma_f32_32x32x16_bf16 v[80:95], v[132:135], v[140:143], v[80:95]\n\t"
      "v_mfma_f32_32x32x16_bf16 v[96:111], v[136:139], v[140:143], v[96:111]\n\t"
      "ds_read_b64_tr_b16 v[132:133], %[wa] offset:11424\n\t"
      "ds_read_b64_tr_b16 v[134:135], %[wa] offset:14240\n\t"
      "ds_read_b64_tr_b16 v[136:137], %[wa] offset:11456\n\t"
      "ds_read_b64_tr_b16 v[138:139], %[wa] offset:14272\n\t"
      "ds_read_b32 v148, %[ca]\n\t"
      "s_mov_b64 exec, %[m56]\n\t"
      "buffer_load_dwordx4 %[va], %[ru], %[so] offen offset:896 lds\n\t"
      "s_mov_b64 exec, -1\n\t"
      "s_add_u32 %[so], %[so], 57344\n\t"
      "s_add_u32 %[thr], %[thr], 1\n\t"
      "s_waitcnt lgkmcnt(9)\n\t"
      "s_cmp_eq_u32 %[thr], 32\n\t"
      "v_mfma_f32_32x32x16_bf16 v[0:15], v[112:115], v[144:147], v[0:15]\n\t"
      "v_mfma_f32_32x32x16_bf16 v[16:31], v[116:119], v[144:147], v[16:31]\n\t"
      "v_mfma_f32_32x32x16_bf16 v[32:47], v[120:123], v[144:147], v[32:47]\n\t"
      "s_cbranch_scc1 5f\n\t"
      "s_waitcnt vmcnt(6) lgkmcnt(0)\n\t"
      "ds_add_u32 %[cw], %[one]\n\t"
      "s_setprio 3\n\t"
      "v_cmp_gt_u32 vcc, %[thr], v148\n\t"
      "s_setprio 0\n\t"
      "s_cbranch_vccnz 11f\n\t"
      "21:\n\t"
      "s_add_i32 m0, %[dst], 0\n\t"
      "ds_read_b128 v[140:143], %[xa] offset:22528\n\t"
      "ds_read_b64_tr_b16 v[112:113], %[wa] offset:22528\n\t"
      "ds_read_b64_tr_b16 v[114:115], %[wa] offset:25344\n\t"
      "s_add_u32 %[sx], %[sx], 1024\n\t"
      "buffer_load_dwordx4 %[vx], %[rx], %[sx] offen offset:1792 sc1 lds\n\t"
      "ds_read_b64_tr_b16 v[116:117], %[wa] offset:22560\n\t"
      "ds_read_b64_tr_b16 v[118:119], %[wa] offset:25376\n\t"
      "v_mfma_f32_32x32x16_bf16 v[48:63], v[124:127], v[144:147], v[48:63]\n\t"
      "v_mfma_f32_32x32x16_bf16 v[64:79], v[128:131], v[144:147], v[64:79]\n\t"
      "ds_read_b64_tr_b16 v[120:121], %[wa] offset:22592\n\t"
      "ds_read_b64_tr_b16 v[122:123], %[wa] offset:25408\n\t"
      "ds_read_b64_tr_b16 v[124:125], %[wa] offset:22624\n\t"
      "ds_read_b64_tr_b16 v[126:127], %[wa] offset:25440\n\t"
      "ds_read_b64_tr_b16 v[128:129], %[wa] offset:22656\n\t"
      "ds_read_b64_tr_b16 v[130:131], %[wa] offset:25472\n\t"
      "s_mov_b64 exec, %[m56]\n\t"
      "buffer_load_dwordx4 %[va], %[rg], %[so] offen lds\n\t"
      "s_mov_b64 exec, -1\n\t"
      "v_mfma_f32_32x32x16_bf16 v[80:95], v[132:135], v[144:147], v[80:95]\n\t"
      "v_mfma_f32_32x32x16_bf16 v[96:111], v[136:139], v[144:147], v[96:111]\n\t"
      "ds_read_b64_tr_b16 v[132:133], %[wa] offset:22688\n\t"
      "ds_read_b64_tr_b16 v[134:135], %[wa] offset:25504\n\t"
      "ds_read_b64_tr_b16 v[136:137], %[wa] offset:22720\n\t"
      "ds_read_b64_tr_b16 v[138:139], %[wa] offset:25536\n\t"
      "ds_read_b32 v148, %[ca]\n\t"
      "s_mov_b64 exec, %[m56]\n\t"
      "buffer_load_dwordx4 %[va], %[ru], %[so] offen offset:896 lds\n\t"
      "s_mov_b64 exec, -1\n\t"
      "s_add_u32 %[so], %[so], 57344\n\t"
      "s_add_u32 %[thr], %[thr], 1\n\t"
      "s_waitcnt lgkmcnt(9)\n\t"
      "v_mfma_f32_32x32x16_bf16 v[0:15], v[112:115], v[140:143], v[0:15]\n\t"
      "v_mfma_f32_32x32x16_bf16 v[16:31], v[116:119], v[140:143], v[16:31]\n\t"
      "v_mfma_f32_32x32x16_bf16 v[32:47], v[120:123], v[140:143], v[32:47]\n\t"
      "s_waitcnt vmcnt(6) lgkmcnt(0)\n\t"
      "ds_add_u32 %[cw], %[one]\n\t"
      "s_setprio 3\n\t"
      "v_cmp_gt_u32 vcc, %[thr], v148\n\t"
      "s_setprio 0\n\t"
      "s_cbranch_vccnz 12f\n\t"
      "22:\n\t"
      "s_add_i32 m0, %[dst], 11264\n\t"
      "ds_read_b128 v[144:147], %[xa] offset:33792\n\t"
      "ds_read_b64_tr_b16 v[112:113], %[wa] offset:33792\n\t"
      "ds_read_b64_tr_b16 v[114:115], %[wa] offset:36608\n\t"
      "s_add_u32 %[sx], %[sx], 1024\n\t"
      "buffer_load_dwordx4 %[vx], %[rx], %[sx] offen offset:1792 sc1 lds\n\t"
      "ds_read_b64_tr_b16 v[116:117], %[wa] offset:33824\n\t"
      "ds_read_b64_tr_b16 v[118:119], %[wa] offset:36640\n\t"
      "v_mfma_f32_32x32x16_bf16 v[48:63], v[124:127], v[140:143], v[48:63]\n\t"
      "v_mfma_f32_32x32x16_bf16 v[64:79], v[128:131], v[140:143], v[64:79]\n\t"
      "ds_read_b64_tr_b16 v[120:121], %[wa] offset:33856\n\t"
      "ds_read_b64_tr_b16 v[122:123], %[wa] offset:36672\n\t"
      "ds_read_b64_tr_b16 v[124:125], %[wa] offset:33888\n\t"
      "ds_read_b64_tr_b16 v[126:127], %[wa] offset:36704\n\t"
      "ds_read_b64_tr_b16 v[128:129], %[wa] offset:33920\n\t"
      "ds_read_b64_tr_b16 v[130:131], %[wa] offset:36736\n\t"
      "s_mov_b64 exec, %[m56]\n\t"
      "buffer_load_dwordx4 %[va], %[rg], %[so] offen lds\n\t"
      "s_mov_b64 exec, -1\n\t"
      "v_mfma_f32_32x32x16_bf16 v[80:95], v[132:135], v[140:143], v[80:95]\n\t"
      "v_mfma_f32_32x32x16_bf16 v[96:111], v[136:139], v[140:143], v[96:111]\n\t"
      "ds_read_b64_tr_b16 v[132:133], %[wa] offset:33952\n\t"
      "ds_read_b64_tr_b16 v[134:135], %[wa] offset:36768\n\t"
      "ds_read_b64_tr_b16 v[136:137], %[wa] offset:33984\n\t"
      "ds_read_b64_tr_b16 v[138:139], %[wa] offset:36800\n\t"
      "ds_read_b32 v148, %[ca]\n\t"
      "s_mov_b64 exec, %[m56]\n\t"
      "buffer_load_dwordx4 %[va], %[ru], %[so] offen offset:896 lds\n\t"
      "s_mov_b64 exec, -1\n\t"
      "s_add_u32 %[so], %[so], 57344\n\t"
      "s_add_u32 %[thr], %[thr], 1\n\t"
      "s_waitcnt lgkmcnt(9)\n\t"
      "v_mfma_f32_32x32x16_bf16 v[0:15], v[112:115], v[144:147], v[0:15]\n\t"
      "v_mfma_f32_32x32x16_bf16 v[16:31], v[116:119], v[144:147], v[16:31]\n\t"
      "v_mfma_f32_32x32x16_bf16 v[32:47], v[120:123], v[144:147], v[32:47]\n\t"
      "s_waitcnt vmcnt(6) lgkmcnt(0)\n\t"
      "ds_add_u32 %[cw], %[one]\n\t"
      "s_setprio 3\n\t"
      "v_cmp_gt_u32 vcc, %[thr], v148\n\t"
      "s_setprio 0\n\t"
      "s_cbranch_vccnz 13f\n\t"
      "23:\n\t"
      "s_add_i32 m0, %[dst], 22528\n\t"
      "ds_read_b128 v[140:143], %[xa] offset:45056\n\t"
      "ds_read_b64_tr_b16 v[112:113], %[wa] offset:45056\n\t"
      "ds_read_b64_tr_b16 v[114:115], %[wa] offset:47872\n\t"
      "s_add_u32 %[sx], %[sx], 1024\n\t"
      "buffer_load_dwordx4 %[vx], %[rx], %[sx] offen offset:1792 sc1 lds\n\t"
      "ds_read_b64_tr_b16 v[116:117], %[wa] offset:45088\n\t"
      "ds_read_b64_tr_b16 v[118:119], %[wa] offset:47904\n\t"
      "v_mfma_f32_32x32x16_bf16 v[48:63], v[124:127], v[144:147], v[48:63]\n\t"
      "v_mfma_f32_32x32x16_bf16 v[64:79], v[128:131], v[144:147], v[64:79]\n\t"
      "ds_read_b64_tr_b16 v[120:121], %[wa] offset:45120\n\t"
      "ds_read_b64_tr_b16 v[122:123], %[wa] offset:47936\n\t"
      "ds_read_b64_tr_b16 v[124:125], %[wa] offset:45152\n\t"
      "ds_read_b64_tr_b16 v[126:127], %[wa] offset:47968\n\t"
      "ds_read_b64_tr_b16 v[128:129], %[wa] offset:45184\n\t"
      "ds_read_b64_tr_b16 v[130:131], %[wa] offset:48000\n\t"
      "s_mov_b64 exec, %[m56]\n\t"
      "buffer_load_dwordx4 %[va], %[rg], %[so] offen lds\n\t"
      "s_mov_b64 exec, -1\n\t"
      "v_mfma_f32_32x32x16_bf16 v[80:95], v[132:135], v[144:147], v[80:95]\n\t"
      "v_mfma_f32_32x32x16_bf16 v[96:111], v[136:139], v[144:147], v[96:111]\n\t"
      "ds_read_b64_tr_b16 v[132:133], %[wa] offset:45216\n\t"
      "ds_read_b64_tr_b16 v[134:135], %[wa] offset:48032\n\t"
      "ds_read_b64_tr_b16 v[136:137], %[wa] offset:45248\n\t"
      "ds_read_b64_tr_b16 v[138:139], %[wa] offset:48064\n\t"
      "ds_read_b32 v148, %[ca]\n\t"
      "s_mov_b64 exec, %[m56]\n\t"
      "buffer_load_dwordx4 %[va], %[ru], %[so] offen offset:896 lds\n\t"
      "s_mov_b64 exec, -1\n\t"
      "s_add_u32 %[so], %[so], 57344\n\t"
      "s_add_u32 %[thr], %[thr], 1\n\t"
      "s_waitcnt lgkmcnt(9)\n\t"
      "v_mfma_f32_32x32x16_bf16 v[0:15], v[112:115], v[140:143], v[0:15]\n\t"
      "v_mfma_f32_32x32x16_bf16 v[16:31], v[116:119], v[140:143], v[16:31]\n\t"
      "v_mfma_f32_32x32x16_bf16 v[32:47], v[120:123], v[140:143], v[32:47]\n\t"
      "s_waitcnt vmcnt(6) lgkmcnt(0)\n\t"
      "ds_add_u32 %[cw], %[one]\n\t"
      "s_setprio 3\n\t"
      "v_cmp_gt_u32 vcc, %[thr], v148\n\t"
      "s_setprio 0\n\t"
      "s_cbranch_vccnz 14f\n\t"
      "24:\n\t"
      "s_add_i32 m0, %[dst], 33792\n\t"
      "ds_read_b128 v[144:147], %[xa] offset:56320\n\t"
      "ds_read_b64_tr_b16 v[112:113], %[wa] offset:56320\n\t"
      "ds_read_b64_tr_b16 v[114:115], %[wa] offset:59136\n\t"
      "s_add_u32 %[sx], %[sx], 1024\n\t"
      "buffer_load_dwordx4 %[vx], %[rx], %[sx] offen offset:1792 sc1 lds\n\t"
      "ds_read_b64_tr_b16 v[116:117], %[wa] offset:56352\n\t"
      "ds_read_b64_tr_b16 v[118:119], %[wa] offset:59168\n\t"
      "v_mfma_f32_32x32x16_bf16 v[48:63], v[124:127], v[140:143], v[48:63]\n\t"
      "v_mfma_f32_32x32x16_bf16 v[64:79], v[128:131], v[140:143], v[64:79]\n\t"
      "ds_read_b64_tr_b16 v[120:121], %[wa] offset:56384\n\t"
      "ds_read_b64_tr_b16 v[122:123], %[wa] offset:59200\n\t"
      "ds_read_b64_tr_b16 v[124:125], %[wa] offset:56416\n\t"
      "ds_read_b64_tr_b16 v[126:127], %[wa] offset:59232\n\t"
      "ds_read_b64_tr_b16 v[128:129], %[wa] offset:56448\n\t"
      "ds_read_b64_tr_b16 v[130:131], %[wa] offset:59264\n\t"
      "s_mov_b64 exec, %[m56]\n\t"
      "buffer_load_dwordx4 %[va], %[rg], %[so] offen lds\n\t"
      "s_mov_b64 exec, -1\n\t"
      "v_mfma_f32_32x32x16_bf16 v[80:95], v[132:135], v[140:143], v[80:95]\n\t"
      "v_mfma_f32_32x32x16_bf16 v[96:111], v[136:139], v[140:143], v[96:111]\n\t"
      "ds_read_b64_tr_b16 v[132:133], %[wa] offset:56480\n\t"
      "ds_read_b64_tr_b16 v[134:135], %[wa] offset:59296\n\t"
      "ds_read_b64_tr_b16 v[136:137], %[wa] offset:56512\n\t"
      "ds_read_b64_tr_b16 v[138:139], %[wa] offset:59328\n\t"
      "ds_read_b32 v148, %[ca]\n\t"
      "s_mov_b64 exec, %[m56]\n\t"
      "buffer_load_dwordx4 %[va], %[ru], %[so] offen offset:896 lds\n\t"
      "s_mov_b64 exec, -1\n\t"
      "s_add_u32 %[so], %[so], 57344\n\t"
      "s_add_u32 %[thr], %[thr], 1\n\t"
      "s_waitcnt lgkmcnt(9)\n\t"
      "v_mfma_f32_32x32x16_bf16 v[0:15], v[112:115], v[144:147], v[0:15]\n\t"
      "v_mfma_f32_32x32x16_bf16 v[16:31], v[116:119], v[144:147], v[16:31]\n\t"
      "v_mfma_f32_32x32x16_bf16 v[32:47], v[120:123], v[144:147], v[32:47]\n\t"
      "s_waitcnt vmcnt(6) lgkmcnt(0)\n\t"
      "ds_add_u32 %[cw], %[one]\n\t"
      "s_setprio 3\n\t"
      "v_cmp_gt_u32 vcc, %[thr], v148\n\t"
      "s_setprio 0\n\t"
      "s_cbranch_vccnz 15f\n\t"
      "25:\n\t"
      "s_add_i32 m0, %[dst], 45056\n\t"
      "ds_read_b128 v[140:143], %[xa] offset:0\n\t"
      "ds_read_b64_tr_b16 v[112:113], %[wa] offset:0\n\t"
      "ds_read_b64_tr_b16 v[114:115], %[wa] offset:2816\n\t"
      "s_add_u32 %[sx], %[sx], 1024\n\t"
      "buffer_load_dwordx4 %[vx], %[rx], %[sx] offen offset:1792 sc1 lds\n\t"
      "ds_read_b64_tr_b16 v[116:117], %[wa] offset:32\n\t"
      "ds_read_b64_tr_b16 v[118:119], %[wa] offset:2848\n\t"
      "v_mfma_f32_32x32x16_bf16 v[48:63], v[124:127], v[144:147], v[48:63]\n\t"
      "v_mfma_f32_32x32x16_bf16 v[64:79], v[128:131], v[144:147], v[64:79]\n\t"
      "ds_read_b64_tr_b16 v[120:121], %[wa] offset:64\n\t"
      "ds_read_b64_tr_b16 v[122:123], %[wa] offset:2880\n\t"
      "ds_read_b64_tr_b16 v[124:125], %[wa] offset:96\n\t"
      "ds_read_b64_tr_b16 v[126:127], %[wa] offset:2912\n\t"
      "ds_read_b64_tr_b16 v[128:129], %[wa] offset:128\n\t"
      "ds_read_b64_tr_b16 v[130:131], %[wa] offset:2944\n\t"
      "s_mov_b64 exec, %[m56]\n\t"
      "buffer_load_dwordx4 %[va], %[rg], %[so] offen lds\n\t"
      "s_mov_b64 exec, -1\n\t"
      "v_mfma_f32_32x32x16_bf16 v[80:95], v[132:135], v[144:147], v[80:95]\n\t"
      "v_mfma_f32_32x32x16_bf16 v[96:111], v[136:139], v[144:147], v[96:111]\n\t"
      "ds_read_b64_tr_b16 v[132:133], %[wa] offset:160\n\t"
      "ds_read_b64_tr_b16 v[134:135], %[wa] offset:2976\n\t"
      "ds_read_b64_tr_b16 v[136:137], %[wa] offset:192\n\t"
      "ds_read_b64_tr_b16 v[138:139], %[wa] offset:3008\n\t"
      "ds_read_b32 v148, %[ca]\n\t"
      "s_mov_b64 exec, %[m56]\n\t"
      "buffer_load_dwordx4 %[va], %[ru], %[so] offen offset:896 lds\n\t"
      "s_mov_b64 exec, -1\n\t"
      "s_add_u32 %[so], %[so], 57344\n\t"
      "s_add_u32 %[thr], %[thr], 1\n\t"
      "s_branch 4b\n\t"
      "10:\n\t"
      "ds_read_b32 v148, %[ca]\n\t"
      "s_add_i32 %[bud], %[bud], -1\n\t"
      "s_cmp_eq_u32 %[bud], 0\n\t"
      "s_cbranch_scc1 20b\n\t"
      "s_waitcnt lgkmcnt(0)\n\t"
      "s_setprio 3\n\t"
      "v_cmp_gt_u32 vcc, %[thr], v148\n\t"
      "s_setprio 0\n\t"
      "s_cbranch_vccz 20b\n\t"
      "s_sleep 1\n\t"
      "s_branch 10b\n\t"
      "11:\n\t"
      "ds_read_b32 v148, %[ca]\n\t"
      "s_add_i32 %[bud], %[bud], -1\n\t"
      "s_cmp_eq_u32 %[bud], 0\n\t"
      "s_cbranch_scc1 21b\n\t"
      "s_waitcnt lgkmcnt(0)\n\t"
      "s_setprio 3\n\t"
      "v_cmp_gt_u32 vcc, %[thr], v148\n\t"
      "s_setprio 0\n\t"
      "s_cbranch_vccz 21b\n\t"
      "s_sleep 1\n\t"
      "s_branch 11b\n\t"
      "12:\n\t"
      "ds_read_b32 v148, %[ca]\n\t"
      "s_add_i32 %[bud], %[bud], -1\n\t"
      "s_cmp_eq_u32 %[bud], 0\n\t"
      "s_cbranch_scc1 22b\n\t"
      "s_waitcnt lgkmcnt(0)\n\t"
      "s_setprio 3\n\t"
      "v_cmp_gt_u32 vcc, %[thr], v148\n\t"
      "s_setprio 0\n\t"
      "s_cbranch_vccz 22b\n\t"
      "s_sleep 1\n\t"
      "s_branch 12b\n\t"
      "13:\n\t"
      "ds_read_b32 v148, %[ca]\n\t"
      "s_add_i32 %[bud], %[bud], -1\n\t"
      "s_cmp_eq_u32 %[bud], 0\n\t"
      "s_cbranch_scc1 23b\n\t"
      "s_waitcnt lgkmcnt(0)\n\t"
      "s_setprio 3\n\t"
      "v_cmp_gt_u32 vcc, %[thr], v148\n\t"
      "s_setprio 0\n\t"
      "s_cbranch_vccz 23b\n\t"
      "s_sleep 1\n\t"
      "s_branch 13b\n\t"
      "14:\n\t"
      "ds_read_b32 v148, %[ca]\n\t"
      "s_add_i32 %[bud], %[bud], -1\n\t"
      "s_cmp_eq_u32 %[bud], 0\n\t"
      "s_cbranch_scc1 24b\n\t"
      "s_waitcnt lgkmcnt(0)\n\t"
      "s_setprio 3\n\t"
      "v_cmp_gt_u32 vcc, %[thr], v148\n\t"
      "s_setprio 0\n\t"
      "s_cbranch_vccz 24b\n\t"
      "s_sleep 1\n\t"
      "s_branch 14b\n\t"
      "15:\n\t"
      "ds_read_b32 v148, %[ca]\n\t"
      "s_add_i32 %[bud], %[bud], -1\n\t"
      "s_cmp_eq_u32 %[bud], 0\n\t"
      "s_cbranch_scc1 25b\n\t"
      "s_waitcnt lgkmcnt(0)\n\t"
      "s_setprio 3\n\t"
      "v_cmp_gt_u32 vcc, %[thr], v148\n\t"
      "s_setprio 0\n\t"
      "s_cbranch_vccz 25b\n\t"
      "s_sleep 1\n\t"
      "s_branch 15b\n\t"
      "5:\n\t"
      "s_waitcnt vmcnt(0) lgkmcnt(0)\n\t"
      "ds_add_u32 %[cw], %[one]\n\t"
      "v_mfma_f32_32x32x16_bf16 v[48:63], v[124:127], v[144:147], v[48:63]\n\t"
      "v_mfma_f32_32x32x16_bf16 v[64:79], v[128:131], v[144:147], v[64:79]\n\t"
      "v_mfma_f32_32x32x16_bf16 v[80:95], v[132:135], v[144:147], v[80:95]\n\t"
      "v_mfma_f32_32x32x16_bf16 v[96:111], v[136:139], v[144:147], v[96:111]\n\t"
      "s_setprio 3\n\t"
      "s_mov_b32 m0, %[keep]\n\t"
      "s_nop 7\n\t"
      "s_nop 3\n\t"
        : "+{v[0:15]}"(acc[0]), "+{v[16:31]}"(acc[1]), "+{v[32:47]}"(acc[2]), "+{v[48:63]}"(acc[3]), "+{v[64:79]}"(acc[4]), "+{v[80:95]}"(acc[5]),
          "+{v[96:111]}"(acc[6]), [so] "+s"(so), [bud] "+s"(bud), [thr] "+s"(thr), [keep] "=&s"(keep), [sx] "+s"(sx)
        : [wa] "v"(wa), [xa] "v"(f1dst + 1792 + l16), [ca] "v"(ca), [cw] "v"(cw), [one] "v"(1u),
          [va] "v"(fa), [vx] "v"(l16), [rg] "s"(rg), [ru] "s"(ru), [rx] "s"(rx), [dst] "s"(f1dst), [m56] "s"(0x00ffffffffffffffull)
        : "memory", "scc", "vcc", "v112", "v113", "v114", "v115", "v116", "v117", "v118", "v119", "v120", "v121", "v122", "v123", "v124", "v125", "v126", "v127",
          "v128", "v129", "v130", "v131", "v132", "v133", "v134", "v135", "v136", "v137", "v138", "v139", "v140", "v141", "v142", "v143", "v144", "v145", "v146",
          "v147", "v148");
      }
      __builtin_amdgcn_s_setprio(3);
      const int lane2 = fresh_lane();
      char* hq = ws + WS_H + ((size_t)(T * 4 + f) * 112 + fg * 7) * 1024;
      u32x4 hold;
#pragma unroll
      for (int j = 0; j < 7; ++j) {
        const float c0 = -2.0f * 0.7978845608028654f * 1.4426950408889634f, c1 = c0 * 0.044715f;
        f32x2 g[4], z[4];
#pragma unroll
        for (int r = 0; r < 4; ++r) { g[r] = f32x2{acc[j][2 * r], acc[j][2 * r + 1]}; z[r] = g[r] * g[r]; }
        SBAR();
#pragma unroll
        for (int r = 0; r < 4; ++r) z[r] = __builtin_elementwise_fma(z[r], f32x2{c1, c1}, f32x2{c0, c0});
        SBAR();
#pragma unroll
        for (int r = 0; r < 4; ++r) z[r] = z[r] * g[r];
        SBAR();
#pragma unroll
        for (int r = 0; r < 4; ++r) z[r] = f32x2{__builtin_amdgcn_exp2f(z[r][0]), __builtin_amdgcn_exp2f(z[r][1])};
        SBAR();
#pragma unroll
        for (int r = 0; r < 4; ++r) z[r] = z[r] + 1.0f;
        SBAR();
#pragma unroll
        for (int r = 0; r < 4; ++r) z[r] = f32x2{__builtin_amdgcn_rcpf(z[r][0]), __builtin_amdgcn_rcpf(z[r][1])};
        SBAR();
#pragma unroll
        for (int r = 0; r < 4; ++r) g[r] = g[r] * z[r];
        SBAR();
#pragma unroll
        for (int r = 0; r < 4; ++r) g[r] = g[r] * f32x2{acc[j][8 + 2 * r], acc[j][9 + 2 * r]};
        SBAR();
        const u32x4 hv = {pk_bf16(g[0][0], g[0][1]), pk_bf16(g[1][0], g[1][1]), pk_bf16(g[2][0], g[2][1]), pk_bf16(g[3][0], g[3][1])};
        if (j == 4) { hold = hv; asm volatile("" : "+v"(hold)); continue; }
        if (j == 5) {
          publish(lds, C_HPUB, 4, ws + WS_FH + (T * 16 + fg) * FS, tag, lane2);
          st16_dev(hq + 4 * 1024, lane2 * 16, hold);
        }
        st16_dev(hq + j * 1024, lane2 * 16, hv);
      }
    }
    LANE_VARS();
    const unsigned va = r8 * (DM * 2) + ((c8 ^ (((r8 >> 1) & 1) << 2)) * 16), vb = va + 8 * DM * 2 - 1024;
    const unsigned ring = lds0 + L_F + f * 16384;
    const unsigned wb0 = ring + (4 * hh + q4) * 128 + ((32 * blk + 8 * p4) ^ ((q4 >> 1) << 6)), wb1 = wb0 ^ 64;
    const u32x4 rw = make_srd((const char*)wd + cg * 128, FF * DM * 2 - cg * 128);
    f32x16 acc[2][2];
#pragma unroll
    for (int i = 0; i < 2; ++i)
#pragma unroll
      for (int j = 0; j < 2; ++j) { acc[i][j] = f32x16{}; asm volatile("" : "+v"(acc[i][j])); }
    prg_wait(lds, lane, 33);
#pragma unroll
    for (int j = 0; j < 8; ++j) bdma2(rw, (f * 28 + (j >> 2) * 7 + (j & 3)) * 16384, va, vb, ring + j * 2048);
    {
      const char* fh = ws + WS_FH + (T * 16 + 4 * f) * FS;
      u64 hf;
      asm volatile("global_load_dwordx2 %0, %1, %2 sc1\n\ts_waitcnt vmcnt(0)" : "=&v"(hf) : "v"((lane & 3) * FS), "s"(fh) : "memory");
      count_and_flag(lds, C_HPUB2, 4, ws + WS_FH + (256 * 3 + T * 16 + fg) * FS, tag, lane);
      if (!__all(hf == tag)) wait_flags(fh + (lane & 3) * FS, lane < 4, tag);
    }
    {
      const u32x4 rh = make_srd(ws + WS_H + ((size_t)tb * 2 * 112 + f * 28) * 1024, 140 * 1024);
      unsigned sh, sw, keep, bud = SPIN_G;
      asm volatile(
      "s_mov_b32 %[keep], m0\n\t"
      "s_setprio 0\n\t"
      "s_movk_i32 %[sh], 0\n\t"
      "buffer_load_dwordx4 v[64:67], %[h0], %[rh], %[sh] offen sc1\n\t"
      "buffer_load_dwordx4 v[68:71], %[h1], %[rh], %[sh] offen sc1\n\t"
      "s_movk_i32 %[sh], 1024\n\t"
      "buffer_load_dwordx4 v[72:75], %[h0], %[rh], %[sh] offen sc1\n\t"
      "buffer_load_dwordx4 v[76:79], %[h1], %[rh], %[sh] offen sc1\n\t"
      "s_movk_i32 %[sh], 2048\n\t"
      "buffer_load_dwordx4 v[80:83], %[h0], %[rh], %[sh] offen sc1\n\t"
      "buffer_load_dwordx4 v[84:87], %[h1], %[rh], %[sh] offen sc1\n\t"
      "s_movk_i32 %[sh], 3072\n\t"
      "buffer_load_dwordx4 v[88:91], %[h0], %[rh], %[sh] offen sc1\n\t"
      "buffer_load_dwordx4 v[92:95], %[h1], %[rh], %[sh] offen sc1\n\t"
      "s_movk_i32 %[sh], 7168\n\t"
      "buffer_load_dwordx4 v[96:99], %[h0], %[rh], %[sh] offen sc1\n\t"
      "buffer_load_dwordx4 v[100:103], %[h1], %[rh], %[sh] offen sc1\n\t"
      "s_movk_i32 %[sh], 8192\n\t"
      "buffer_load_dwordx4 v[104:107], %[h0], %[rh], %[sh] offen sc1\n\t"
      "buffer_load_dwordx4 v[108:111], %[h1], %[rh], %[sh] offen sc1\n\t"
      "s_movk_i32 %[sh], 9216\n\t"
      "buffer_load_dwordx4 v[112:115], %[h0], %[rh], %[sh] offen sc1\n\t"
      "buffer_load_dwordx4 v[116:119], %[h1], %[rh], %[sh] offen sc1\n\t"
      "s_movk_i32 %[sh], 10240\n\t"
      "buffer_load_dwordx4 v[120:123], %[h0], %[rh], %[sh] offen sc1\n\t"
      "buffer_load_dwordx4 v[124:127], %[h1], %[rh], %[sh] offen sc1\n\t"
      "s_waitcnt vmcnt(14)\n\t"
      "ds_read_b64_tr_b16 v[128:129], %[wb0] offset:0\n\t"
      "ds_read_b64_tr_b16 v[130:131], %[wb0] offset:1024\n\t"
      "ds_read_b64_tr_b16 v[132:133], %[wb1] offset:0\n\t"
      "ds_read_b64_tr_b16 v[134:135], %[wb1] offset:1024\n\t"
      "s_waitcnt lgkmcnt(0)\n\t"
      "v_mfma_f32_32x32x16_bf16 v[0:15], v[128:131], v[64:67], v[0:15]\n\t"
      "v_mfma_f32_32x32x16_bf16 v[16:31], v[132:135], v[64:67], v[16:31]\n\t"
      "s_waitcnt vmcnt(12)\n\t"
      "ds_read_b64_tr_b16 v[136:137], %[wb0] offset:2048\n\t"
      "ds_read_b64_tr_b16 v[138:139], %[wb0] offset:3072\n\t"
      "ds_read_b64_tr_b16 v[140:141], %[wb1] offset:2048\n\t"
      "ds_read_b64_tr_b16 v[142:143], %[wb1] offset:3072\n\t"
      "s_add_i32 m0, %[ring], 0\n\t"
      "s_add_i32 %[sw], %[sw0], 229376\n\t"
      "buffer_load_dwordx4 %[va], %[rw], %[sw] offen lds\n\t"
      "buffer_load_dwordx4 %[vb], %[rw], %[sw] offen offset:1024 lds\n\t"
      "v_mfma_f32_32x32x16_bf16 v[32:47], v[128:131], v[68:71], v[32:47]\n\t"
      "v_mfma_f32_32x32x16_bf16 v[48:63], v[132:135], v[68:71], v[48:63]\n\t"
      "s_movk_i32 %[sh], 14336\n\t"
      "buffer_load_dwordx4 v[64:67], %[h0], %[rh], %[sh] offen sc1\n\t"
      "buffer_load_dwordx4 v[68:71], %[h1], %[rh], %[sh] offen sc1\n\t"
      "s_waitcnt lgkmcnt(0)\n\t"
      "v_mfma_f32_32x32x16_bf16 v[0:15], v[136:139], v[72:75], v[0:15]\n\t"
      "v_mfma_f32_32x32x16_bf16 v[16:31], v[140:143], v[72:75], v[16:31]\n\t"
      "s_waitcnt vmcnt(14)\n\t"
      "ds_read_b64_tr_b16 v[128:129], %[wb0] offset:4096\n\t"
      "ds_read_b64_tr_b16 v[130:131], %[wb0] offset:5120\n\t"
      "ds_read_b64_tr_b16 v[132:133], %[wb1] offset:4096\n\t"
      "ds_read_b64_tr_b16 v[134:135], %[wb1] offset:5120\n\t"
      "s_add_i32 m0, %[ring], 2048\n\t"
      "s_add_i32 %[sw], %[sw0], 245760\n\t"
      "buffer_load_dwordx4 %[va], %[rw], %[sw] offen lds\n\t"
      "buffer_load_dwordx4 %[vb], %[rw], %[sw] offen offset:1024 lds\n\t"
      "v_mfma_f32_32x32x16_bf16 v[32:47], v[136:139], v[76:79], v[32:47]\n\t"
      "v_mfma_f32_32x32x16_bf16 v[48:63], v[140:143], v[76:79], v[48:63]\n\t"
      "s_movk_i32 %[sh], 15360\n\t"
      "buffer_load_dwordx4 v[72:75], %[h0], %[rh], %[sh] offen sc1\n\t"
      "buffer_load_dwordx4 v[76:79], %[h1], %[rh], %[sh] offen sc1\n\t"
      "s_waitcnt lgkmcnt(0)\n\t"
      "v_mfma_f32_32x32x16_bf16 v[0:15], v[128:131], v[80:83], v[0:15]\n\t"
      "v_mfma_f32_32x32x16_bf16 v[16:31], v[132:135], v[80:83], v[16:31]\n\t"
      "s_waitcnt vmcnt(16)\n\t"
      "ds_read_b64_tr_b16 v[136:137], %[wb0] offset:6144\n\t"
      "ds_read_b64_tr_b16 v[138:139], %[wb0] offset:7168\n\t"
      "ds_read_b64_tr_b16 v[140:141], %[wb1] offset:6144\n\t"
      "ds_read_b64_tr_b16 v[142:143], %[wb1] offset:7168\n\t"
      "s_add_i32 m0, %[ring], 4096\n\t"
      "s_add_i32 %[sw], %[sw0], 262144\n\t"
      "buffer_load_dwordx4 %[va], %[rw], %[sw] offen lds\n\t"
      "buffer_load_dwordx4 %[vb], %[rw], %[sw] offen offset:1024 lds\n\t"
      "v_mfma_f32_32x32x16_bf16 v[32:47], v[128:131], v[84:87], v[32:47]\n\t"
      "v_mfma_f32_32x32x16_bf16 v[48:63], v[132:135], v[84:87], v[48:63]\n\t"
      "s_movk_i32 %[sh], 16384\n\t"
      "buffer_load_dwordx4 v[80:83], %[h0], %[rh], %[sh] offen sc1\n\t"
      "buffer_load_dwordx4 v[84:87], %[h1], %[rh], %[sh] offen sc1\n\t"
      "s_waitcnt lgkmcnt(0)\n\t"
      "v_mfma_f32_32x32x16_bf16 v[0:15], v[136:139], v[88:91], v[0:15]\n\t"
      "v_mfma_f32_32x32x16_bf16 v[16:31], v[140:143], v[88:91], v[16:31]\n\t"
      "s_waitcnt vmcnt(18)\n\t"
      "ds_read_b64_tr_b16 v[128:129], %[wb0] offset:8192\n\t"
      "ds_read_b64_tr_b16 v[130:131], %[wb0] offset:9216\n\t"
      "ds_read_b64_tr_b16 v[132:133], %[wb1] offset:8192\n\t"
      "ds_read_b64_tr_b16 v[134:135], %[wb1] offset:9216\n\t"
      "s_add_i32 m0, %[ring], 6144\n\t"
      "s_add_i32 %[sw], %[sw0], 278528\n\t"
      "buffer_load_dwordx4 %[va], %[rw], %[sw] offen lds\n\t"
      "buffer_load_dwordx4 %[vb], %[rw], %[sw] offen offset:1024 lds\n\t"
      "v_mfma_f32_32x32x16_bf16 v[32:47], v[136:139], v[92:95], v[32:47]\n\t"
      "v_mfma_f32_32x32x16_bf16 v[48:63], v[140:143], v[92:95], v[48:63]\n\t"
      "s_movk_i32 %[sh], 17408\n\t"
      "buffer_load_dwordx4 v[88:91], %[h0], %[rh], %[sh] offen sc1\n\t"
      "buffer_load_dwordx4 v[92:95], %[h1], %[rh], %[sh] offen sc1\n\t"
      "global_load_dwordx2 v[144:145], %[fo], %[fb] sc1\n\t"
      "s_waitcnt lgkmcnt(0)\n\t"
      "v_mfma_f32_32x32x16_bf16 v[0:15], v[128:131], v[96:99], v[0:15]\n\t"
      "v_mfma_f32_32x32x16_bf16 v[16:31], v[132:135], v[96:99], v[16:31]\n\t"
      "s_waitcnt vmcnt(21)\n\t"
      "ds_read_b64_tr_b16 v[136:137], %[wb0] offset:10240\n\t"
      "ds_read_b64_tr_b16 v[138:139], %[wb0] offset:11264\n\t"
      "ds_read_b64_tr_b16 v[140:141], %[wb1] offset:10240\n\t"
      "ds_read_b64_tr_b16 v[142:143], %[wb1] offset:11264\n\t"
      "s_add_i32 m0, %[ring], 8192\n\t"
      "s_add_i32 %[sw], %[sw0], 344064\n\t"
      "buffer_load_dwordx4 %[va], %[rw], %[sw] offen lds\n\t"
      "buffer_load_dwordx4 %[vb], %[rw], %[sw] offen offset:1024 lds\n\t"
      "v_mfma_f32_32x32x16_bf16 v[32:47], v[128:131], v[100:103], v[32:47]\n\t"
      "v_mfma_f32_32x32x16_bf16 v[48:63], v[132:135], v[100:103], v[48:63]\n\t"
      "s_movk_i32 %[sh], 21504\n\t"
      "buffer_load_dwordx4 v[96:99], %[h0], %[rh], %[sh] offen sc1\n\t"
      "buffer_load_dwordx4 v[100:103], %[h1], %[rh], %[sh] offen sc1\n\t"
      "s_waitcnt lgkmcnt(0)\n\t"
      "v_mfma_f32_32x32x16_bf16 v[0:15], v[136:139], v[104:107], v[0:15]\n\t"
      "v_mfma_f32_32x32x16_bf16 v[16:31], v[140:143], v[104:107], v[16:31]\n\t"
      "s_waitcnt vmcnt(23)\n\t"
      "ds_read_b64_tr_b16 v[128:129], %[wb0] offset:12288\n\t"
      "ds_read_b64_tr_b16 v[130:131], %[wb0] offset:13312\n\t"
      "ds_read_b64_tr_b16 v[132:133], %[wb1] offset:12288\n\t"
      "ds_read_b64_tr_b16 v[134:135], %[wb1] offset:13312\n\t"
      "s_add_i32 m0, %[ring], 10240\n\t"
      "s_add_i32 %[sw], %[sw0], 360448\n\t"
      "buffer_load_dwordx4 %[va], %[rw], %[sw] offen lds\n\t"
      "buffer_load_dwordx4 %[vb], %[rw], %[sw] offen offset:1024 lds\n\t"
      "v_mfma_f32_32x32x16_bf16 v[32:47], v[136:139], v[108:111], v[32:47]\n\t"
      "v_mfma_f32_32x32x16_bf16 v[48:63], v[140:143], v[108:111], v[48:63]\n\t"
      "s_movk_i32 %[sh], 22528\n\t"
      "buffer_load_dwordx4 v[104:107], %[h0], %[rh], %[sh] offen sc1\n\t"
      "buffer_load_dwordx4 v[108:111], %[h1], %[rh], %[sh] offen sc1\n\t"
      "s_waitcnt lgkmcnt(0)\n\t"
      "v_mfma_f32_32x32x16_bf16 v[0:15], v[128:131], v[112:115], v[0:15]\n\t"
      "v_mfma_f32_32x32x16_bf16 v[16:31], v[132:135], v[112:115], v[16:31]\n\t"
      "s_waitcnt vmcnt(25)\n\t"
      "ds_read_b64_tr_b16 v[136:137], %[wb0] offset:14336\n\t"
      "ds_read_b64_tr_b16 v[138:139], %[wb0] offset:15360\n\t"
      "ds_read_b64_tr_b16 v[140:141], %[wb1] offset:14336\n\t"
      "ds_read_b64_tr_b16 v[142:143], %[wb1] offset:15360\n\t"
      "s_add_i32 m0, %[ring], 12288\n\t"
      "s_add_i32 %[sw], %[sw0], 376832\n\t"
      "buffer_load_dwordx4 %[va], %[rw], %[sw] offen lds\n\t"
      "buffer_load_dwordx4 %[vb], %[rw], %[sw] offen offset:1024 lds\n\t"
      "v_mfma_f32_32x32x16_bf16 v[32:47], v[128:131], v[116:119], v[32:47]\n\t"
      "v_mfma_f32_32x32x16_bf16 v[48:63], v[132:135], v[116:119], v[48:63]\n\t"
      "s_movk_i32 %[sh], 23552\n\t"
      "buffer_load_dwordx4 v[112:115], %[h0], %[rh], %[sh] offen sc1\n\t"
      "buffer_load_dwordx4 v[116:119], %[h1], %[rh], %[sh] offen sc1\n\t"
      "s_waitcnt lgkmcnt(0)\n\t"
      "v_mfma_f32_32x32x16_bf16 v[0:15], v[136:139], v[120:123], v[0:15]\n\t"
      "v_mfma_f32_32x32x16_bf16 v[16:31], v[140:143], v[120:123], v[16:31]\n\t"
      "s_waitcnt vmcnt(12)\n\t"
      "ds_read_b64_tr_b16 v[128:129], %[wb0] offset:0\n\t"
      "ds_read_b64_tr_b16 v[130:131], %[wb0] offset:1024\n\t"
      "ds_read_b64_tr_b16 v[132:133], %[wb1] offset:0\n\t"
      "ds_read_b64_tr_b16 v[134:135], %[wb1] offset:1024\n\t"
      "s_setprio 3\n\t"
      "v_cmp_ne_u64 vcc, %[tag], v[144:145]\n\t"
      "s_setprio 0\n\t"
      "s_cbranch_vccnz 7f\n\t"
      "8:\n\t"
      "s_add_i32 m0, %[ring], 14336\n\t"
      "s_add_i32 %[sw], %[sw0], 393216\n\t"
      "buffer_load_dwordx4 %[va], %[rw], %[sw] offen lds\n\t"
      "buffer_load_dwordx4 %[vb], %[rw], %[sw] offen offset:1024 lds\n\t"
      "v_mfma_f32_32x32x16_bf16 v[32:47], v[136:139], v[124:127], v[32:47]\n\t"
      "v_mfma_f32_32x32x16_bf16 v[48:63], v[140:143], v[124:127], v[48:63]\n\t"
      "s_movk_i32 %[sh], 24576\n\t"
      "buffer_load_dwordx4 v[120:123], %[h0], %[rh], %[sh] offen sc1\n\t"
      "buffer_load_dwordx4 v[124:127], %[h1], %[rh], %[sh] offen sc1\n\t"
      "s_waitcnt lgkmcnt(0)\n\t"
      "v_mfma_f32_32x32x16_bf16 v[0:15], v[128:131], v[64:67], v[0:15]\n\t"
      "v_mfma_f32_32x32x16_bf16 v[16:31], v[132:135], v[64:67], v[16:31]\n\t"
      "s_waitcnt vmcnt(25)\n\t"
      "ds_read_b64_tr_b16 v[136:137], %[wb0] offset:2048\n\t"
      "ds_read_b64_tr_b16 v[138:139], %[wb0] offset:3072\n\t"
      "ds_read_b64_tr_b16 v[140:141], %[wb1] offset:2048\n\t"
      "ds_read_b64_tr_b16 v[142:143], %[wb1] offset:3072\n\t"
      "s_add_i32 m0, %[ring], 0\n\t"
      "s_add_i32 %[sw], %[sw0], 65536\n\t"
      "buffer_load_dwordx4 %[va], %[rw], %[sw] offen lds\n\t"
      "buffer_load_dwordx4 %[vb], %[rw], %[sw] offen offset:1024 lds\n\t"
      "v_mfma_f32_32x32x16_bf16 v[32:47], v[128:131], v[68:71], v[32:47]\n\t"
      "v_mfma_f32_32x32x16_bf16 v[48:63], v[132:135], v[68:71], v[48:63]\n\t"
      "s_movk_i32 %[sh], 4096\n\t"
      "buffer_load_dwordx4 v[64:67], %[h0], %[rh], %[sh] offen sc1\n\t"
      "buffer_load_dwordx4 v[68:71], %[h1], %[rh], %[sh] offen sc1\n\t"
      "s_waitcnt lgkmcnt(0)\n\t"
      "v_mfma_f32_32x32x16_bf16 v[0:15], v[136:139], v[72:75], v[0:15]\n\t"
      "v_mfma_f32_32x32x16_bf16 v[16:31], v[140:143], v[72:75], v[16:31]\n\t"
      "s_waitcnt vmcnt(25)\n\t"
      "ds_read_b64_tr_b16 v[128:129], %[wb0] offset:4096\n\t"
      "ds_read_b64_tr_b16 v[130:131], %[wb0] offset:5120\n\t"
      "ds_read_b64_tr_b16 v[132:133], %[wb1] offset:4096\n\t"
      "ds_read_b64_tr_b16 v[134:135], %[wb1] offset:5120\n\t"
      "s_add_i32 m0, %[ring], 2048\n\t"
      "s_add_i32 %[sw], %[sw0], 81920\n\t"
      "buffer_load_dwordx4 %[va], %[rw], %[sw] offen lds\n\t"
      "buffer_load_dwordx4 %[vb], %[rw], %[sw] offen offset:1024 lds\n\t"
      "v_mfma_f32_32x32x16_bf16 v[32:47], v[136:139], v[76:79], v[32:47]\n\t"
      "v_mfma_f32_32x32x16_bf16 v[48:63], v[140:143], v[76:79], v[48:63]\n\t"
      "s_movk_i32 %[sh], 5120\n\t"
      "buffer_load_dwordx4 v[72:75], %[h0], %[rh], %[sh] offen sc1\n\t"
      "buffer_load_dwordx4 v[76:79], %[h1], %[rh], %[sh] offen sc1\n\t"
      "s_waitcnt lgkmcnt(0)\n\t"
      "v_mfma_f32_32x32x16_bf16 v[0:15], v[128:131], v[80:83], v[0:15]\n\t"
      "v_mfma_f32_32x32x16_bf16 v[16:31], v[132:135], v[80:83], v[16:31]\n\t"
      "s_waitcnt vmcnt(25)\n\t"
      "ds_read_b64_tr_b16 v[136:137], %[wb0] offset:6144\n\t"
      "ds_read_b64_tr_b16 v[138:139], %[wb0] offset:7168\n\t"
      "ds_read_b64_tr_b16 v[140:141], %[wb1] offset:6144\n\t"
      "ds_read_b64_tr_b16 v[142:143], %[wb1] offset:7168\n\t"
      "s_add_i32 m0, %[ring], 4096\n\t"
      "s_add_i32 %[sw], %[sw0], 98304\n\t"
      "buffer_load_dwordx4 %[va], %[rw], %[sw] offen lds\n\t"
      "buffer_load_dwordx4 %[vb], %[rw], %[sw] offen offset:1024 lds\n\t"
      "v_mfma_f32_32x32x16_bf16 v[32:47], v[128:131], v[84:87], v[32:47]\n\t"
      "v_mfma_f32_32x32x16_bf16 v[48:63], v[132:135], v[84:87], v[48:63]\n\t"
      "s_movk_i32 %[sh], 6144\n\t"
      "buffer_load_dwordx4 v[80:83], %[h0], %[rh], %[sh] offen sc1\n\t"
      "buffer_load_dwordx4 v[84:87], %[h1], %[rh], %[sh] offen sc1\n\t"
      "s_waitcnt lgkmcnt(0)\n\t"
      "v_mfma_f32_32x32x16_bf16 v[0:15], v[136:139], v[88:91], v[0:15]\n\t"
      "v_mfma_f32_32x32x16_bf16 v[16:31], v[140:143], v[88:91], v[16:31]\n\t"
      "s_waitcnt vmcnt(24)\n\t"
      "ds_read_b64_tr_b16 v[128:129], %[wb0] offset:8192\n\t"
      "ds_read_b64_tr_b16 v[130:131], %[wb0] offset:9216\n\t"
      "ds_read_b64_tr_b16 v[132:133], %[wb1] offset:8192\n\t"
      "ds_read_b64_tr_b16 v[134:135], %[wb1] offset:9216\n\t"
      "s_add_i32 m0, %[ring], 6144\n\t"
      "s_add_i32 %[sw], %[sw0], 180224\n\t"
      "buffer_load_dwordx4 %[va], %[rw], %[sw] offen lds\n\t"
      "buffer_load_dwordx4 %[vb], %[rw], %[sw] offen offset:1024 lds\n\t"
      "v_mfma_f32_32x32x16_bf16 v[32:47], v[136:139], v[92:95], v[32:47]\n\t"
      "v_mfma_f32_32x32x16_bf16 v[48:63], v[140:143], v[92:95], v[48:63]\n\t"
      "s_movk_i32 %[sh], 11264\n\t"
      "buffer_load_dwordx4 v[88:91], %[h0], %[rh], %[sh] offen sc1\n\t"
      "buffer_load_dwordx4 v[92:95], %[h1], %[rh], %[sh] offen sc1\n\t"
      "s_waitcnt lgkmcnt(0)\n\t"
      "v_mfma_f32_32x32x16_bf16 v[0:15], v[128:131], v[96:99], v[0:15]\n\t"
      "v_mfma_f32_32x32x16_bf16 v[16:31], v[132:135], v[96:99], v[16:31]\n\t"
      "s_waitcnt vmcnt(24)\n\t"
      "ds_read_b64_tr_b16 v[136:137], %[wb0] offset:10240\n\t"
      "ds_read_b64_tr_b16 v[138:139], %[wb0] offset:11264\n\t"
      "ds_read_b64_tr_b16 v[140:141], %[wb1] offset:10240\n\t"
      "ds_read_b64_tr_b16 v[142:143], %[wb1] offset:11264\n\t"
      "s_add_i32 m0, %[ring], 8192\n\t"
      "s_add_i32 %[sw], %[sw0], 196608\n\t"
      "buffer_load_dwordx4 %[va], %[rw], %[sw] offen lds\n\t"
      "buffer_load_dwordx4 %[vb], %[rw], %[sw] offen offset:1024 lds\n\t"
      "v_mfma_f32_32x32x16_bf16 v[32:47], v[128:131], v[100:103], v[32:47]\n\t"
      "v_mfma_f32_32x32x16_bf16 v[48:63], v[132:135], v[100:103], v[48:63]\n\t"
      "s_movk_i32 %[sh], 12288\n\t"
      "buffer_load_dwordx4 v[96:99], %[h0], %[rh], %[sh] offen sc1\n\t"
      "buffer_load_dwordx4 v[100:103], %[h1], %[rh], %[sh] offen sc1\n\t"
      "s_waitcnt lgkmcnt(0)\n\t"
      "v_mfma_f32_32x32x16_bf16 v[0:15], v[136:139], v[104:107], v[0:15]\n\t"
      "v_mfma_f32_32x32x16_bf16 v[16:31], v[140:143], v[104:107], v[16:31]\n\t"
      "s_waitcnt vmcnt(24)\n\t"
      "ds_read_b64_tr_b16 v[128:129], %[wb0] offset:12288\n\t"
      "ds_read_b64_tr_b16 v[130:131], %[wb0] offset:13312\n\t"
      "ds_read_b64_tr_b16 v[132:133], %[wb1] offset:12288\n\t"
      "ds_read_b64_tr_b16 v[134:135], %[wb1] offset:13312\n\t"
      "s_add_i32 m0, %[ring], 10240\n\t"
      "s_add_i32 %[sw], %[sw0], 212992\n\t"
      "buffer_load_dwordx4 %[va], %[rw], %[sw] offen lds\n\t"
      "buffer_load_dwordx4 %[vb], %[rw], %[sw] offen offset:1024 lds\n\t"
      "v_mfma_f32_32x32x16_bf16 v[32:47], v[136:139], v[108:111], v[32:47]\n\t"
      "v_mfma_f32_32x32x16_bf16 v[48:63], v[140:143], v[108:111], v[48:63]\n\t"
      "s_movk_i32 %[sh], 13312\n\t"
      "buffer_load_dwordx4 v[104:107], %[h0], %[rh], %[sh] offen sc1\n\t"
      "buffer_load_dwordx4 v[108:111], %[h1], %[rh], %[sh] offen sc1\n\t"
      "s_waitcnt lgkmcnt(0)\n\t"
      "v_mfma_f32_32x32x16_bf16 v[0:15], v[128:131], v[112:115], v[0:15]\n\t"
      "v_mfma_f32_32x32x16_bf16 v[16:31], v[132:135], v[112:115], v[16:31]\n\t"
      "s_waitcnt vmcnt(24)\n\t"
      "ds_read_b64_tr_b16 v[136:137], %[wb0] offset:14336\n\t"
      "ds_read_b64_tr_b16 v[138:139], %[wb0] offset:15360\n\t"
      "ds_read_b64_tr_b16 v[140:141], %[wb1] offset:14336\n\t"
      "ds_read_b64_tr_b16 v[142:143], %[wb1] offset:15360\n\t"
      "s_add_i32 m0, %[ring], 12288\n\t"
      "s_add_i32 %[sw], %[sw0], 294912\n\t"
      "buffer_load_dwordx4 %[va], %[rw], %[sw] offen lds\n\t"
      "buffer_load_dwordx4 %[vb], %[rw], %[sw] offen offset:1024 lds\n\t"
      "v_mfma_f32_32x32x16_bf16 v[32:47], v[128:131], v[116:119], v[32:47]\n\t"
      "v_mfma_f32_32x32x16_bf16 v[48:63], v[132:135], v[116:119], v[48:63]\n\t"
      "s_movk_i32 %[sh], 18432\n\t"
      "buffer_load_dwordx4 v[112:115], %[h0], %[rh], %[sh] offen sc1\n\t"
      "buffer_load_dwordx4 v[116:119], %[h1], %[rh], %[sh] offen sc1\n\t"
      "s_waitcnt lgkmcnt(0)\n\t"
      "v_mfma_f32_32x32x16_bf16 v[0:15], v[136:139], v[120:123], v[0:15]\n\t"
      "v_mfma_f32_32x32x16_bf16 v[16:31], v[140:143], v[120:123], v[16:31]\n\t"
      "s_waitcnt vmcnt(24)\n\t"
      "ds_read_b64_tr_b16 v[128:129], %[wb0] offset:0\n\t"
      "ds_read_b64_tr_b16 v[130:131], %[wb0] offset:1024\n\t"
      "ds_read_b64_tr_b16 v[132:133], %[wb1] offset:0\n\t"
      "ds_read_b64_tr_b16 v[134:135], %[wb1] offset:1024\n\t"
      "s_add_i32 m0, %[ring], 14336\n\t"
      "s_add_i32 %[sw], %[sw0], 311296\n\t"
      "buffer_load_dwordx4 %[va], %[rw], %[sw] offen lds\n\t"
      "buffer_load_dwordx4 %[vb], %[rw], %[sw] offen offset:1024 lds\n\t"
      "v_mfma_f32_32x32x16_bf16 v[32:47], v[136:139], v[124:127], v[32:47]\n\t"
      "v_mfma_f32_32x32x16_bf16 v[48:63], v[140:143], v[124:127], v[48:63]\n\t"
      "s_movk_i32 %[sh], 19456\n\t"
      "buffer_load_dwordx4 v[120:123], %[h0], %[rh], %[sh] offen sc1\n\t"
      "buffer_load_dwordx4 v[124:127], %[h1], %[rh], %[sh] offen sc1\n\t"
      "s_waitcnt lgkmcnt(0)\n\t"
      "v_mfma_f32_32x32x16_bf16 v[0:15], v[128:131], v[64:67], v[0:15]\n\t"
      "v_mfma_f32_32x32x16_bf16 v[16:31], v[132:135], v[64:67], v[16:31]\n\t"
      "s_waitcnt vmcnt(24)\n\t"
      "ds_read_b64_tr_b16 v[136:137], %[wb0] offset:2048\n\t"
      "ds_read_b64_tr_b16 v[138:139], %[wb0] offset:3072\n\t"
      "ds_read_b64_tr_b16 v[140:141], %[wb1] offset:2048\n\t"
      "ds_read_b64_tr_b16 v[142:143], %[wb1] offset:3072\n\t"
      "s_add_i32 m0, %[ring], 0\n\t"
      "s_add_i32 %[sw], %[sw0], 327680\n\t"
      "buffer_load_dwordx4 %[va], %[rw], %[sw] offen lds\n\t"
      "buffer_load_dwordx4 %[vb], %[rw], %[sw] offen offset:1024 lds\n\t"
      "v_mfma_f32_32x32x16_bf16 v[32:47], v[128:131], v[68:71], v[32:47]\n\t"
      "v_mfma_f32_32x32x16_bf16 v[48:63], v[132:135], v[68:71], v[48:63]\n\t"
      "s_movk_i32 %[sh], 20480\n\t"
      "buffer_load_dwordx4 v[64:67], %[h0], %[rh], %[sh] offen sc1\n\t"
      "buffer_load_dwordx4 v[68:71], %[h1], %[rh], %[sh] offen sc1\n\t"
      "s_waitcnt lgkmcnt(0)\n\t"
      "v_mfma_f32_32x32x16_bf16 v[0:15], v[136:139], v[72:75], v[0:15]\n\t"
      "v_mfma_f32_32x32x16_bf16 v[16:31], v[140:143], v[72:75], v[16:31]\n\t"
      "s_waitcnt vmcnt(24)\n\t"
      "ds_read_b64_tr_b16 v[128:129], %[wb0] offset:4096\n\t"
      "ds_read_b64_tr_b16 v[130:131], %[wb0] offset:5120\n\t"
      "ds_read_b64_tr_b16 v[132:133], %[wb1] offset:4096\n\t"
      "ds_read_b64_tr_b16 v[134:135], %[wb1] offset:5120\n\t"
      "s_add_i32 m0, %[ring], 2048\n\t"
      "s_add_i32 %[sw], %[sw0], 409600\n\t"
      "buffer_load_dwordx4 %[va], %[rw], %[sw] offen lds\n\t"
      "buffer_load_dwordx4 %[vb], %[rw], %[sw] offen offset:1024 lds\n\t"
      "v_mfma_f32_32x32x16_bf16 v[32:47], v[136:139], v[76:79], v[32:47]\n\t"
      "v_mfma_f32_32x32x16_bf16 v[48:63], v[140:143], v[76:79], v[48:63]\n\t"
      "s_movk_i32 %[sh], 25600\n\t"
      "buffer_load_dwordx4 v[72:75], %[h0], %[rh], %[sh] offen sc1\n\t"
      "buffer_load_dwordx4 v[76:79], %[h1], %[rh], %[sh] offen sc1\n\t"
      "s_waitcnt lgkmcnt(0)\n\t"
      "v_mfma_f32_32x32x16_bf16 v[0:15], v[128:131], v[80:83], v[0:15]\n\t"
      "v_mfma_f32_32x32x16_bf16 v[16:31], v[132:135], v[80:83], v[16:31]\n\t"
      "s_waitcnt vmcnt(24)\n\t"
      "ds_read_b64_tr_b16 v[136:137], %[wb0] offset:6144\n\t"
      "ds_read_b64_tr_b16 v[138:139], %[wb0] offset:7168\n\t"
      "ds_read_b64_tr_b16 v[140:141], %[wb1] offset:6144\n\t"
      "ds_read_b64_tr_b16 v[142:143], %[wb1] offset:7168\n\t"
      "s_add_i32 m0, %[ring], 4096\n\t"
      "s_add_i32 %[sw], %[sw0], 425984\n\t"
      "buffer_load_dwordx4 %[va], %[rw], %[sw] offen lds\n\t"
      "buffer_load_dwordx4 %[vb], %[rw], %[sw] offen offset:1024 lds\n\t"
      "v_mfma_f32_32x32x16_bf16 v[32:47], v[128:131], v[84:87], v[32:47]\n\t"
      "v_mfma_f32_32x32x16_bf16 v[48:63], v[132:135], v[84:87], v[48:63]\n\t"
      "s_movk_i32 %[sh], 26624\n\t"
      "buffer_load_dwordx4 v[80:83], %[h0], %[rh], %[sh] offen sc1\n\t"
      "buffer_load_dwordx4 v[84:87], %[h1], %[rh], %[sh] offen sc1\n\t"
      "s_waitcnt lgkmcnt(0)\n\t"
      "v_mfma_f32_32x32x16_bf16 v[0:15], v[136:139], v[88:91], v[0:15]\n\t"
      "v_mfma_f32_32x32x16_bf16 v[16:31], v[140:143], v[88:91], v[16:31]\n\t"
      "s_waitcnt vmcnt(24)\n\t"
      "ds_read_b64_tr_b16 v[128:129], %[wb0] offset:8192\n\t"
      "ds_read_b64_tr_b16 v[130:131], %[wb0] offset:9216\n\t"
      "ds_read_b64_tr_b16 v[132:133], %[wb1] offset:8192\n\t"
      "ds_read_b64_tr_b16 v[134:135], %[wb1] offset:9216\n\t"
      "s_add_i32 m0, %[ring], 6144\n\t"
      "s_add_i32 %[sw], %[sw0], 442368\n\t"
      "buffer_load_dwordx4 %[va], %[rw], %[sw] offen lds\n\t"
      "buffer_load_dwordx4 %[vb], %[rw], %[sw] offen offset:1024 lds\n\t"
      "v_mfma_f32_32x32x16_bf16 v[32:47], v[136:139], v[92:95], v[32:47]\n\t"
      "v_mfma_f32_32x32x16_bf16 v[48:63], v[140:143], v[92:95], v[48:63]\n\t"
      "s_movk_i32 %[sh], 27648\n\t"
      "buffer_load_dwordx4 v[88:91], %[h0], %[rh], %[sh] offen sc1\n\t"
      "buffer_load_dwordx4 v[92:95], %[h1], %[rh], %[sh] offen sc1\n\t"
      "s_waitcnt lgkmcnt(0)\n\t"
      "v_mfma_f32_32x32x16_bf16 v[0:15], v[128:131], v[96:99], v[0:15]\n\t"
      "v_mfma_f32_32x32x16_bf16 v[16:31], v[132:135], v[96:99], v[16:31]\n\t"
      "s_waitcnt vmcnt(24)\n\t"
      "ds_read_b64_tr_b16 v[136:137], %[wb0] offset:10240\n\t"
      "ds_read_b64_tr_b16 v[138:139], %[wb0] offset:11264\n\t"
      "ds_read_b64_tr_b16 v[140:141], %[wb1] offset:10240\n\t"
      "ds_read_b64_tr_b16 v[142:143], %[wb1] offset:11264\n\t"
      "v_mfma_f32_32x32x16_bf16 v[32:47], v[128:131], v[100:103], v[32:47]\n\t"
      "v_mfma_f32_32x32x16_bf16 v[48:63], v[132:135], v[100:103], v[48:63]\n\t"
      "s_waitcnt lgkmcnt(0)\n\t"
      "v_mfma_f32_32x32x16_bf16 v[0:15], v[136:139], v[104:107], v[0:15]\n\t"
      "v_mfma_f32_32x32x16_bf16 v[16:31], v[140:143], v[104:107], v[16:31]\n\t"
      "s_waitcnt vmcnt(20)\n\t"
      "ds_read_b64_tr_b16 v[128:129], %[wb0] offset:12288\n\t"
      "ds_read_b64_tr_b16 v[130:131], %[wb0] offset:13312\n\t"
      "ds_read_b64_tr_b16 v[132:133], %[wb1] offset:12288\n\t"
      "ds_read_b64_tr_b16 v[134:135], %[wb1] offset:13312\n\t"
      "v_mfma_f32_32x32x16_bf16 v[32:47], v[136:139], v[108:111], v[32:47]\n\t"
      "v_mfma_f32_32x32x16_bf16 v[48:63], v[140:143], v[108:111], v[48:63]\n\t"
      "s_waitcnt lgkmcnt(0)\n\t"
      "v_mfma_f32_32x32x16_bf16 v[0:15], v[128:131], v[112:115], v[0:15]\n\t"
      "v_mfma_f32_32x32x16_bf16 v[16:31], v[132:135], v[112:115], v[16:31]\n\t"
      "s_waitcnt vmcnt(16)\n\t"
      "ds_read_b64_tr_b16 v[136:137], %[wb0] offset:14336\n\t"
      "ds_read_b64_tr_b16 v[138:139], %[wb0] offset:15360\n\t"
      "ds_read_b64_tr_b16 v[140:141], %[wb1] offset:14336\n\t"
      "ds_read_b64_tr_b16 v[142:143], %[wb1] offset:15360\n\t"
      "v_mfma_f32_32x32x16_bf16 v[32:47], v[128:131], v[116:119], v[32:47]\n\t"
      "v_mfma_f32_32x32x16_bf16 v[48:63], v[132:135], v[116:119], v[48:63]\n\t"
      "s_waitcnt lgkmcnt(0)\n\t"
      "v_mfma_f32_32x32x16_bf16 v[0:15], v[136:139], v[120:123], v[0:15]\n\t"
      "v_mfma_f32_32x32x16_bf16 v[16:31], v[140:143], v[120:123], v[16:31]\n\t"
      "s_waitcnt vmcnt(12)\n\t"
      "ds_read_b64_tr_b16 v[128:129], %[wb0] offset:0\n\t"
      "ds_read_b64_tr_b16 v[130:131], %[wb0] offset:1024\n\t"
      "ds_read_b64_tr_b16 v[132:133], %[wb1] offset:0\n\t"
      "ds_read_b64_tr_b16 v[134:135], %[wb1] offset:1024\n\t"
      "v_mfma_f32_32x32x16_bf16 v[32:47], v[136:139], v[124:127], v[32:47]\n\t"
      "v_mfma_f32_32x32x16_bf16 v[48:63], v[140:143], v[124:127], v[48:63]\n\t"
      "s_waitcnt lgkmcnt(0)\n\t"
      "v_mfma_f32_32x32x16_bf16 v[0:15], v[128:131], v[64:67], v[0:15]\n\t"
      "v_mfma_f32_32x32x16_bf16 v[16:31], v[132:135], v[64:67], v[16:31]\n\t"
      "s_waitcnt vmcnt(8)\n\t"
      "ds_read_b64_tr_b16 v[136:137], %[wb0] offset:2048\n\t"
      "ds_read_b64_tr_b16 v[138:139], %[wb0] offset:3072\n\t"
      "ds_read_b64_tr_b16 v[140:141], %[wb1] offset:2048\n\t"
      "ds_read_b64_tr_b16 v[142:143], %[wb1] offset:3072\n\t"
      "v_mfma_f32_32x32x16_bf16 v[32:47], v[128:131], v[68:71], v[32:47]\n\t"
      "v_mfma_f32_32x32x16_bf16 v[48:63], v[132:135], v[68:71], v[48:63]\n\t"
      "s_waitcnt lgkmcnt(0)\n\t"
      "v_mfma_f32_32x32x16_bf16 v[0:15], v[136:139], v[72:75], v[0:15]\n\t"
      "v_mfma_f32_32x32x16_bf16 v[16:31], v[140:143], v[72:75], v[16:31]\n\t"
      "s_waitcnt vmcnt(4)\n\t"
      "ds_read_b64_tr_b16 v[128:129], %[wb0] offset:4096\n\t"
      "ds_read_b64_tr_b16 v[130:131], %[wb0] offset:5120\n\t"
      "ds_read_b64_tr_b16 v[132:133], %[wb1] offset:4096\n\t"
      "ds_read_b64_tr_b16 v[134:135], %[wb1] offset:5120\n\t"
      "v_mfma_f32_32x32x16_bf16 v[32:47], v[136:139], v[76:79], v[32:47]\n\t"
      "v_mfma_f32_32x32x16_bf16 v[48:63], v[140:143], v[76:79], v[48:63]\n\t"
      "s_waitcnt lgkmcnt(0)\n\t"
      "v_mfma_f32_32x32x16_bf16 v[0:15], v[128:131], v[80:83], v[0:15]\n\t"
      "v_mfma_f32_32x32x16_bf16 v[16:31], v[132:135], v[80:83], v[16:31]\n\t"
      "s_waitcnt vmcnt(0)\n\t"
      "ds_read_b64_tr_b16 v[136:137], %[wb0] offset:6144\n\t"
      "ds_read_b64_tr_b16 v[138:139], %[wb0] offset:7168\n\t"
      "ds_read_b64_tr_b16 v[140:141], %[wb1] offset:6144\n\t"
      "ds_read_b64_tr_b16 v[142:143], %[wb1] offset:7168\n\t"
      "v_mfma_f32_32x32x16_bf16 v[32:47], v[128:131], v[84:87], v[32:47]\n\t"
      "v_mfma_f32_32x32x16_bf16 v[48:63], v[132:135], v[84:87], v[48:63]\n\t"
      "s_waitcnt lgkmcnt(0)\n\t"
      "v_mfma_f32_32x32x16_bf16 v[0:15], v[136:139], v[88:91], v[0:15]\n\t"
      "v_mfma_f32_32x32x16_bf16 v[16:31], v[140:143], v[88:91], v[16:31]\n\t"
      "v_mfma_f32_32x32x16_bf16 v[32:47], v[136:139], v[92:95], v[32:47]\n\t"
      "v_mfma_f32_32x32x16_bf16 v[48:63], v[140:143], v[92:95], v[48:63]\n\t"
      "s_branch 9f\n\t"
      "7:\n\t"
      "global_load_dwordx2 v[144:145], %[fo], %[fb] sc1\n\t"
      "s_add_i32 %[bud], %[bud], -1\n\t"
      "s_cmp_eq_u32 %[bud], 0\n\t"
      "s_cbranch_scc1 8b\n\t"
      "s_waitcnt vmcnt(0)\n\t"
      "s_setprio 3\n\t"
      "v_cmp_ne_u64 vcc, %[tag], v[144:145]\n\t"
      "s_setprio 0\n\t"
      "s_cbranch_vccz 8b\n\t"
      "s_sleep 1\n\t"
      "s_branch 7b\n\t"
      "9:\n\t"
      "s_setprio 3\n\t"
      "s_mov_b32 m0, %[keep]\n\t"
      "s_nop 7\n\t"
      "s_nop 3\n\t"
      : "+{v[0:15]}"(acc[0][0]), "+{v[16:31]}"(acc[0][1]), "+{v[32:47]}"(acc[1][0]), "+{v[48:63]}"(acc[1][1]), [sh] "=&s"(sh), [sw] "=&s"(sw), [keep] "=&s"(keep), [bud] "+s"(bud)
      : [wb0] "v"(wb0), [wb1] "v"(wb1), [va] "v"(va), [vb] "v"(vb), [h0] "v"(l16), [h1] "v"(l16 + 112 * 1024), [rw] "s"(rw), [rh] "s"(rh),
        [ring] "s"(ring), [sw0] "s"(f * 28 * 16384), [tag] "s"(tag), [fo] "v"((lane & 3) * FS + 256 * 3 * FS), [fb] "s"(ws + WS_FH + (T * 16 + 4 * f) * FS)
      : F2_CLOB, "vcc", "v144", "v145");
    }
    __builtin_amdgcn_s_setprio(3);
    write_partial(lds + L_F + f * 16384, acc, l31, hh);
    BARRIER();
  }
}

extern "C" void kernel_launch(void* const* d_in, const int* in_sizes, int n_in, void* d_out, int out_size, void* d_ws, size_t ws_size,
                              hipStream_t stream) {
  (void)hipFuncSetAttribute((const void*)k_fused, hipFuncAttributeMaxDynamicSharedMemorySize, LDS_BYTES);
  k_fused<<<256, 768, LDS_BYTES, stream>>>((const uint16_t*)d_in[0], (const uint16_t*)d_in[1], (const uint16_t*)d_in[2], (const uint16_t*)d_in[4],
                                           (const uint16_t*)d_in[5], (const uint16_t*)d_in[6], (uint16_t*)d_out, (char*)d_ws);
}
```
